```python
import torch

def _softmax(x, axis=-1):
    return torch.softmax(x, dim=axis)
def _gelu(x):
    return torch.nn.functional.gelu(x, approximate="tanh")
def _silu(x):
    return torch.nn.functional.silu(x)
def _sigmoid(x):
    return torch.sigmoid(x)
def _layernorm(x, axis=-1, eps=1e-5):
    m = x.mean(dim=axis, keepdim=True)
    v = x.var(dim=axis, keepdim=True, unbiased=False)
    return (x - m) / torch.sqrt(v + eps)
def _scatter_add(tgt, idx, src, axis=0):
    return tgt.clone().index_add_(axis, idx, src)
def _segment_sum(src, seg, n):
    out = torch.zeros((n,) + src.shape[1:], dtype=src.dtype)
    return out.index_add_(0, seg, src)
def _pad(x, pw):
    flat = tuple(v for pair in reversed(pw) for v in pair)
    return torch.nn.functional.pad(x, flat)

kernel_name = 'recurrent_scan__ssm_conv_s4__dense_activation_mish__prompt'

def setup_inputs():

    _w = (lambda a: (lambda t: t.to(torch.bfloat16) if t.is_floating_point() else t)(torch.as_tensor(a)))
    def _A_setup():
        B, L, D, N = 1, 2048, 640, 192
        A_re = -(lambda _x: torch.exp(torch.as_tensor(_x)))((lambda *s: torch.randn(*s, dtype=torch.bfloat16) * 3.2)(D, N))
        A_im = torch.pi * (lambda *s: torch.randn(*s, dtype=torch.bfloat16) * 0.2)(D, N)
        B_re = (lambda *s: torch.randn(*s, dtype=torch.bfloat16) * 0.2)(D, N)
        B_im = (lambda *s: torch.randn(*s, dtype=torch.bfloat16) * 0.2)(D, N)
        C_re = (lambda *s: torch.randn(*s, dtype=torch.bfloat16) * 0.2)(D, N)
        C_im = (lambda *s: torch.randn(*s, dtype=torch.bfloat16) * 0.2)(D, N)
        log_dt = (lambda *s: torch.randn(*s, dtype=torch.bfloat16) * 0.2)(D) * 0.5 - 4.0
        Dskip = (lambda *s: torch.randn(*s, dtype=torch.bfloat16) * 0.2)(D)
        return {
            "u": _w((lambda *s: torch.randn(*s, dtype=torch.bfloat16) * 0.05)(B, L, D)),
            "A_re": _w(A_re),
            "A_im": _w(A_im),
            "B_re": _w(B_re),
            "B_im": _w(B_im),
            "C_re": _w(C_re),
            "C_im": _w(C_im),
            "log_dt": _w(log_dt),
            "Dskip": _w(Dskip),
        }
    _A_in = _A_setup()

    def _B_setup():
        B, S, D, F = 1, 2048, 640, 1536
        return {
            "x": _w((lambda *s: torch.randn(*s, dtype=torch.bfloat16) * 0.05)(B, S, D)),
            "w1": _w((lambda *s: torch.randn(*s, dtype=torch.bfloat16) * 0.8)(D, F) * (D ** -0.5)),
            "b1": _w((lambda *s: torch.randn(*s, dtype=torch.bfloat16) * 0.1)(F)),
            "w2": _w((lambda *s: torch.randn(*s, dtype=torch.bfloat16) * 0.1)(F, D) * (F ** -0.5)),
        }
    _B_in = _B_setup()

    return {
        "A_u": _A_in["u"],
        "A_A_re": _A_in["A_re"],
        "A_A_im": _A_in["A_im"],
        "A_B_re": _A_in["B_re"],
        "A_B_im": _A_in["B_im"],
        "A_C_re": _A_in["C_re"],
        "A_C_im": _A_in["C_im"],
        "A_log_dt": _A_in["log_dt"],
        "A_Dskip": _A_in["Dskip"],
        "B_x": _B_in["x"],
        "B_w1": _B_in["w1"],
        "B_b1": _B_in["b1"],
        "B_w2": _B_in["w2"],
    }

def reference(A_u, A_A_re, A_A_im, A_B_re, A_B_im, A_C_re, A_C_im, A_log_dt, A_Dskip, B_x, B_w1, B_b1, B_w2):
    def _A_ref(u, A_re, A_im, B_re, B_im, C_re, C_im, log_dt, Dskip):
        Bsz, L, D = u.shape
        dt = torch.exp(log_dt)[:, None]
        A = A_re + 1j * A_im
        Bc = B_re + 1j * B_im
        Cc = C_re + 1j * C_im
        logdA = dt * A
        dB = dt * Bc
        l = (lambda x, dt: x.to(dt))(torch.arange(L), A_re.dtype)
        P = torch.exp(logdA[:, :, None] * l[None, None, :])
        K = torch.einsum("dn,dnl,dn->dl", Cc, P, dB).real
        L2 = 2 * L
        Kp = _pad(K, ((0, 0), (0, L)))
        up = (lambda x, *d: x.permute(*d))(u, 0, 2, 1)
        up = _pad(up, ((0, 0), (0, 0), (0, L)))
        Kf = (lambda x, axes=(-2,-1): torch.fft.rfft2(x.float(), dim=axes))(Kp[:, None, :], axes=(-2, -1))
        Uf = (lambda x, axes=(-2,-1): torch.fft.rfft2(x.float(), dim=axes))(up[:, :, None, :], axes=(-2, -1))
        Yf = Uf * Kf[None, :, :, :]
        y = (lambda x, s, axes=(-2,-1): torch.fft.irfft2(x, s=s, dim=axes).to(torch.bfloat16))(Yf, (1, L2), axes=(-2, -1))[:, :, 0, :L]
        y = (lambda x, *d: x.permute(*d))(y, 0, 2, 1) + u * Dskip[None, None, :]
        return y

    def _B_ref(x, w1, b1, w2):
        h = torch.einsum("bsd,df->bsf", x, w1) + b1
        sp = torch.log1p(torch.exp(-torch.abs(h))) + torch.where(h > 0, h, torch.zeros_like(h))
        act = h * torch.tanh(sp)
        return torch.einsum("bsf,fd->bsd", act, w2)

    x = A_u
    xn = _layernorm(x, axis=-1)
    s = _A_ref(xn, A_A_re, A_A_im, A_B_re, A_B_im, A_C_re, A_C_im, A_log_dt, A_Dskip)
    g = _silu(_B_ref(xn, B_w1, B_b1, B_w2))
    return x + s * g
```

```cpp
#include <hip/hip_runtime.h>
#include <hip/hip_bf16.h>
#include <stdint.h>
#define ROUND_L 0
constexpr int L = 2048, D = 640, N = 192, F = 1536;
typedef unsigned short u16;
__device__ __forceinline__ float bf2f(u16 v) { return __uint_as_float(((unsigned)v) << 16); }
__device__ __forceinline__ u16 f2bf(float f) { unsigned u = __float_as_uint(f); return (u16)((u + 0x7FFF + ((u >> 16) & 1)) >> 16); }
__device__ __forceinline__ float rb(float f) { return bf2f(f2bf(f)); }

__global__ void ln_k(const u16* x, float* xn) {
  int l = blockIdx.x; int t = threadIdx.x;
  float s = 0;
  for (int i = t; i < D; i += 64) s += bf2f(x[l * D + i]);
  for (int o = 32; o; o >>= 1) s += __shfl_xor(s, o);
  float mean = s / D;
  float q = 0;
  for (int i = t; i < D; i += 64) { float v = bf2f(x[l * D + i]) - mean; q += v * v; }
  for (int o = 32; o; o >>= 1) q += __shfl_xor(q, o);
  float var = rb(q / D);
  float m = rb(mean);
  float sd = rb(sqrtf(rb(var + 1e-5f)));
  for (int i = t; i < D; i += 64) xn[l * D + i] = rb(rb(bf2f(x[l * D + i]) - m) / sd);
}
__global__ void kgen_k(const u16* Are, const u16* Aim, const u16* Bre, const u16* Bim, const u16* Cre, const u16* Cim, const u16* logdt, float* K) {
  int d = blockIdx.y; int l = blockIdx.x * 256 + threadIdx.x;
  float dt = rb(expf(bf2f(logdt[d])));
  float lf = ROUND_L ? rb((float)l) : (float)l;
  float acc = 0;
  for (int n = 0; n < N; n++) {
    float ar = dt * bf2f(Are[d * N + n]), ai = dt * bf2f(Aim[d * N + n]);
    float br = dt * bf2f(Bre[d * N + n]), bi = dt * bf2f(Bim[d * N + n]);
    float cr = bf2f(Cre[d * N + n]), ci = bf2f(Cim[d * N + n]);
    float wr = cr * br - ci * bi, wi = cr * bi + ci * br;
    float e = expf(ar * lf); float sn, cs; sincosf(ai * lf, &sn, &cs);
    acc += e * (wr * cs - wi * sn);
  }
  K[d * L + l] = acc;
}
__global__ void conv_k(const float* K, const float* xn, const u16* Dskip, float* s) {
  int l = blockIdx.y; int d = blockIdx.x * 64 + threadIdx.x;
  float acc = 0;
  for (int j = 0; j <= l; j++) acc += K[d * L + j] * xn[(l - j) * D + d];
  s[l * D + d] = rb(rb(acc) + rb(xn[l * D + d] * bf2f(Dskip[d])));
}
__global__ void g1_k(const float* xn, const u16* w1, const u16* b1, float* act) {
  int l = blockIdx.y; int f = blockIdx.x * 256 + threadIdx.x;
  float acc = 0;
  for (int k = 0; k < D; k++) acc += xn[l * D + k] * bf2f(w1[k * F + f]);
  float h = rb(rb(acc) + bf2f(b1[f]));
  float sp = log1pf(expf(-fabsf(h))) + (h > 0 ? h : 0.f);
  act[l * F + f] = rb(h * tanhf(sp));
}
__global__ void g2_k(const float* act, const u16* w2, const float* s, const u16* x, u16* out) {
  int l = blockIdx.y; int d = blockIdx.x * 64 + threadIdx.x;
  float acc = 0;
  for (int k = 0; k < F; k++) acc += act[l * F + k] * bf2f(w2[k * D + d]);
  float o = rb(acc);
  float g = rb(o / (1.f + expf(-o)));
  out[l * D + d] = f2bf(bf2f(x[l * D + d]) + rb(s[l * D + d] * g));
}
extern "C" void kernel_launch(void* const* d_in, const int* in_sizes, int n_in, void* d_out, int out_size, void* d_ws, size_t ws_size, hipStream_t stream) {
  const u16* x = (const u16*)d_in[0];
  float* xn = (float*)d_ws; float* K = xn + L * D; float* s = K + D * L; float* act = s + L * D;
  ln_k<<<L, 64, 0, stream>>>(x, xn);
  kgen_k<<<dim3(L / 256, D), 256, 0, stream>>>((const u16*)d_in[1], (const u16*)d_in[2], (const u16*)d_in[3], (const u16*)d_in[4], (const u16*)d_in[5], (const u16*)d_in[6], (const u16*)d_in[7], K);
  conv_k<<<dim3(D / 64, L), 64, 0, stream>>>(K, xn, (const u16*)d_in[8], s);
  g1_k<<<dim3(F / 256, L), 256, 0, stream>>>(xn, (const u16*)d_in[10], (const u16*)d_in[11], act);
  g2_k<<<dim3(D / 64, L), 64, 0, stream>>>(act, (const u16*)d_in[12], s, x, (u16*)d_out);
}
```

```cpp
#define PROBE 0
#define PROLE (wave < 4)
#define A_SLEEP 4
#include <hip/hip_runtime.h>
#include <stdint.h>
#include <utility>

typedef unsigned short u16;
typedef unsigned u32;
typedef unsigned long long u64;
typedef __attribute__((ext_vector_type(2))) float f32x2;
typedef __attribute__((ext_vector_type(4))) float f32x4;
typedef __attribute__((ext_vector_type(16))) float f32x16;
typedef __attribute__((ext_vector_type(4))) short s16x4;
typedef __attribute__((ext_vector_type(2))) __bf16 bf16x2;
typedef __attribute__((ext_vector_type(8))) __bf16 bf16x8;
typedef __attribute__((ext_vector_type(2))) _Float16 f16x2;
typedef __attribute__((ext_vector_type(8))) _Float16 f16x8;
typedef __attribute__((ext_vector_type(2))) u32 u32x2;
typedef __attribute__((ext_vector_type(4))) u32 u32x4;
typedef __amdgpu_buffer_rsrc_t rsrc_t;
#define DI __device__ __forceinline__
#define LAS __attribute__((address_space(3)))
#define SPIN_NAP()
#define CFENCE() asm volatile("" ::: "memory")
#define SB() __builtin_amdgcn_sched_barrier(0)

constexpr u32 REP = 65536 + 256;
constexpr u32 WS_EPOCH = 0, WS_STATS = 16384, WS_STG = 32768, WS_CSP = 65536, WS_FB = (14u << 20) + 524288, WS_FA = 15u << 20, WS_F1 = WS_FA + 4096, WS_FC = WS_FA + 24576,
              WS_W2X = 131072, WS_XT = 1u << 20, WS_ST = 4u << 20, WS_ACT = 8u << 20, WS_DBG = 16u << 20, WS_USED = 24u << 20;
constexpr u32 L_SSM = 0, SSM_SZ = 15488, L_XS = L_SSM + 3 * SSM_SZ, L_CS = L_XS + 10240, L_B1 = L_CS + 384, L_ST = L_B1 + 384,
              L_SY = L_ST + 1024, L_DUMMY = L_SY + 512, L_END = L_DUMMY + 512, RB = 61440;
constexpr u32 SY_M = L_SY, SY_TAG = L_SY + 16, SY_TAGOK = L_SY + 24, SY_EPI1 = L_SY + 80, SY_U = L_SY + 96, SY_FULL = L_SY + 36,
              SY_EPI2 = L_SY + 40, SY_W2X = L_SY + 44, SY_SDONE = L_SY + 48, SY_RED = L_SY + 64, SY_XF = L_SY + 112, SY_RS = L_SY + 224,
              SY_F0 = L_SY + 256, SY_KD = L_SY + 272, SY_B = L_SY + 288, SY_F = L_SY + 128;
constexpr u32 L_SIMG = RB, L_RED = RB + 16384 + 4096;
static_assert((L_RED & 16383) + 10240 <= 16384 && L_RED + 16384 + 10240 <= RB + 49152, "red");
static_assert(L_END <= RB && RB + 102400 <= 163840, "lds");
constexpr u32 SPIN_CAP = 1u << 17, LSPIN_CAP = 1u << 22;
#define W2_PF_AT 7
#define PK_ROUNDS 1
#define POLL_AT 7
constexpr float LOG2E = 1.4426950408889634f, LN2 = 0.6931471805599453f;

constexpr u32 T4 = 5376, T8 = 5888, S5 = 16, S3 = 3216, S9 = 6400, S7 = 8752, LB_DUM = 7168;
__shared__ __attribute__((aligned(1024))) char smem[163840];

template <class F, int... I> DI void sfor_(F&& f, std::integer_sequence<int, I...>) { (f(std::integral_constant<int, I>{}), ...); }
template <int N, class F> DI void sfor(F&& f) { sfor_(f, std::make_integer_sequence<int, N>{}); }
template <class T> DI LAS T* lp(u32 off) { return (LAS T*)((LAS char*)smem + off); }
DI u32 opq(u32 x) { asm volatile("" : "+v"(x)); return x; }
DI u32 rfl(u32 x) { return __builtin_amdgcn_readfirstlane(x); }
DI float bflo(u32 v) { return __uint_as_float(v << 16); }
DI float bfhi(u32 v) { return __uint_as_float(v & 0xffff0000u); }
DI u32 pk2(float a, float b) { bf16x2 v = {(__bf16)a, (__bf16)b}; return __builtin_bit_cast(u32, v); }
DI float rbf(float a) { return (float)(__bf16)a; }
DI rsrc_t mk(const void* p, u32 n) { return __builtin_amdgcn_make_buffer_rsrc((void*)p, (short)0, (int)n, 0x00020000); }
DI u32x4 ld16c(rsrc_t r, u32 vo, u32 so) { return __builtin_amdgcn_raw_buffer_load_b128(r, vo, so, 16); }
DI u32x2 ld8c(rsrc_t r, u32 vo, u32 so) { return __builtin_amdgcn_raw_buffer_load_b64(r, vo, so, 16); }
DI u32 ld4c(rsrc_t r, u32 vo, u32 so) { return __builtin_amdgcn_raw_buffer_load_b32(r, vo, so, 16); }
DI void st16c(u32x4 v, rsrc_t r, u32 vo, u32 so) { __builtin_amdgcn_raw_buffer_store_b128(v, r, vo + so, 0, 16); }
DI void st16(u32x4 v, rsrc_t r, u32 vo, u32 so) { __builtin_amdgcn_raw_buffer_store_b128(v, r, vo + so, 0, 0); }
DI void st8c(u32x2 v, rsrc_t r, u32 vo, u32 so) { __builtin_amdgcn_raw_buffer_store_b64(v, r, vo, so, 16); }
DI void st4c(u32 v, rsrc_t r, u32 vo, u32 so) { __builtin_amdgcn_raw_buffer_store_b32(v, r, vo, so, 16); }
DI u32x4 ld16(rsrc_t r, u32 vo, u32 so) { return __builtin_amdgcn_raw_buffer_load_b128(r, vo, so, 0); }
#define VMCNT(n) asm volatile("s_waitcnt vmcnt(" #n ")" ::: "memory")

DI void dma(u32 lds, u32 vo, rsrc_t r, u32 so) {
  asm volatile("s_mov_b32 m0, %0\n\ts_nop 0\n\tbuffer_load_dwordx4 %1, %2, %3 offen lds" ::"s"(lds), "v"(vo), "s"(r), "s"(so));
}
template <int OFF> DI void dma_next(u32 vo, rsrc_t r, u32 so) {
  asm volatile("buffer_load_dwordx4 %0, %1, %2 offen offset:%3 lds" ::"v"(vo), "s"(r), "s"(so), "i"(OFF));
}
DI void dma_flag(u32 lds, rsrc_t r) {
  asm volatile("s_mov_b64 exec, 1\n\ts_mov_b32 m0, %0\n\ts_nop 0\n\tbuffer_load_dword off, %1, 0 lds\n\ts_mov_b64 exec, -1" ::"s"(lds), "s"(r) : "memory");
}
DI void dma3(u32 lds, u32 v0, u32 v1, u32 v2, rsrc_t r, u32 so) {
  asm volatile("s_mov_b32 m0, %0\n\ts_nop 0\n\tbuffer_load_dwordx4 %1, %4, %5 offen lds\n\tbuffer_load_dwordx4 %2, %4, %5 offen offset:1024 lds\n\t"
               "buffer_load_dwordx4 %3, %4, %5 offen offset:2048 lds" ::"s"(lds), "v"(v0), "v"(v1), "v"(v2), "s"(r), "s"(so));
}
DI void dma4(u32 lds, u32 v0, u32 v1, u32 v2, u32 v3, rsrc_t r, u32 so) {
  asm volatile("s_mov_b32 m0, %0\n\ts_nop 0\n\tbuffer_load_dwordx4 %1, %5, %6 offen lds\n\tbuffer_load_dwordx4 %2, %5, %6 offen offset:1024 lds\n\t"
               "buffer_load_dwordx4 %3, %5, %6 offen offset:2048 lds\n\tbuffer_load_dwordx4 %4, %5, %6 offen offset:3072 lds"
               ::"s"(lds), "v"(v0), "v"(v1), "v"(v2), "v"(v3), "s"(r), "s"(so));
}
DI void dmac(u32 lds, u32 vo, rsrc_t r, u32 so) {
  asm volatile("s_mov_b32 m0, %0\n\ts_nop 0\n\tbuffer_load_dwordx4 %1, %2, %3 offen sc1 lds" ::"s"(lds), "v"(vo), "s"(r), "s"(so));
}
DI void dma2c(u32 lds, u32 vo, rsrc_t r, u32 so) {
  asm volatile("s_mov_b32 m0, %0\n\ts_nop 0\n\tbuffer_load_dwordx4 %1, %2, %3 offen sc1 lds\n\tbuffer_load_dwordx4 %1, %2, %3 offen offset:1024 sc1 lds"
               ::"s"(lds), "v"(vo), "s"(r), "s"(so));
}
DI u32x2 trd(u32 off) { return __builtin_bit_cast(u32x2, __builtin_amdgcn_ds_read_tr16_b64_v4i16(lp<s16x4>(off))); }
DI bf16x8 frag(u32x2 a, u32x2 b) { u32x4 v = {a.x, a.y, b.x, b.y}; return __builtin_bit_cast(bf16x8, v); }
DI bf16x8 fragv(u32x4 v) { return __builtin_bit_cast(bf16x8, v); }
DI f32x4 mf16(bf16x8 a, bf16x8 b, f32x4 c) { return __builtin_amdgcn_mfma_f32_16x16x32_bf16(a, b, c, 0, 0, 0); }
DI f32x16 mf32(bf16x8 a, bf16x8 b, f32x16 c) { return __builtin_amdgcn_mfma_f32_32x32x16_bf16(a, b, c, 0, 0, 0); }

DI void lds_set(u32 off, u32 v) { CFENCE(); *(volatile LAS u32*)lp<u32>(off) = v; CFENCE(); }
DI void lds_wait(u32 off, u32 v) {
  CFENCE();
#pragma nounroll
  for (u32 n = 0; rfl(*(volatile LAS u32*)lp<u32>(off)) < v && n < LSPIN_CAP; n++) __builtin_amdgcn_s_sleep(1);
  CFENCE();
}
DI u32 lds_min4(u32 off) {
  CFENCE();
  u32x4 x = *(volatile LAS u32x4*)lp<u32x4>(off);
  u32 m = rfl(min(min(x.x, x.y), min(x.z, x.w)));
  CFENCE();
  return m;
}
DI void lds_wait4(u32 off, u32 v) {
#pragma nounroll
  for (u32 n = 0; lds_min4(off) < v && n < LSPIN_CAP; n++) __builtin_amdgcn_s_sleep(1);
}
DI void lds_spin4(u32 off, u32 v) {
#pragma nounroll
  for (u32 n = 0; lds_min4(off) < v && n < LSPIN_CAP; n++) { SPIN_NAP(); }
}
DI u32x2 get_tag() { lds_wait(SY_TAGOK, 1); return *lp<u32x2>(SY_TAG); }
#define A_DELAY 16
#define C_DELAY 12
#define PARK_AT 12
#define G2_WIN 7
#define XT_PACE 2
#define ST_PACE 0
#ifndef M_PRIO
#define M_PRIO 0
#endif
#define S_PRIO 1
#define L_DELAY 4
#define P_DELAY 10
#define BULK_DELAY 5
#define VMX "32"
#define B_SLEEP 4
#define C_SLEEP 8
constexpr u32 OOR = 0x7ffffff0u;
template <int NL, int SLEEP> DI void poll_flags(rsrc_t ws, u32 (&off)[NL], u32 so, u32x2 tag) {
  u64 pend[NL];
#pragma unroll
  for (int i = 0; i < NL; i++) pend[i] = 1;
#pragma nounroll
  for (u32 spin = 0; spin < SPIN_CAP; spin++) {
    CFENCE();
    u32x2 v[NL];
#pragma unroll
    for (int i = 0; i < NL; i++) { v[i] = tag; if (NL == 1 || pend[i]) v[i] = ld8c(ws, off[i], so); }
    u64 any = 0;
#pragma unroll
    for (int i = 0; i < NL; i++) {
      if (v[i].x == tag.x && v[i].y == tag.y) off[i] = OOR;
      pend[i] = __builtin_amdgcn_ballot_w64(off[i] != OOR);
      any |= pend[i];
    }
    if (any == 0) break;
    __builtin_amdgcn_s_sleep(SLEEP);
  }
  CFENCE();
}
template <int NL, int SLEEP> DI void poll_packed(rsrc_t ws, u32 vo, u32 so, u32x2 tag) {
  u32 off[NL];
#pragma unroll
  for (int i = 0; i < NL; i++) off[i] = vo + i * 1024;
#pragma nounroll
  for (u32 spin = 0; spin < SPIN_CAP; spin++) {
    CFENCE();
    u32x4 v[NL];
#pragma unroll
    for (int i = 0; i < NL; i++) v[i] = ld16c(ws, off[i], so);
    bool pend = false;
#pragma unroll
    for (int i = 0; i < NL; i++) {
      if (v[i].x == tag.x && v[i].y == tag.y && v[i].z == tag.x && v[i].w == tag.y) off[i] = OOR;
      pend = pend || off[i] != OOR;
    }
    if (__builtin_amdgcn_ballot_w64(pend) == 0) break;
    __builtin_amdgcn_s_sleep(SLEEP);
  }
  CFENCE();
}
template <int R> DI void publish(rsrc_t ws, u32 so, u32x2 tag, int lane) {
  VMCNT(0);
  st8c(tag, ws, lane < R ? lane * REP : OOR, so);
  CFENCE();
}

#define STAMP(i) do { if ((i) == PROBE) prb_ = (u32)(__builtin_amdgcn_s_memrealtime() - rt0_); } while (0)
#define STAMPS_DECL const u64 rt0_ = __builtin_amdgcn_s_memrealtime(); u32 prb_ = 0
#define STAMPS_GO
#define STAMPS_OUT() do { const u32 ex_ = (PROLE) ? prb_ : 0; VMCNT(0); while (__builtin_amdgcn_s_memrealtime() - rt0_ < 3500 + ex_) __builtin_amdgcn_s_sleep(2); } while (0)

template <bool PK> DI f32x2 cmul(f32x2 a, f32x2 b) {
  if constexpr (PK) {
    f32x2 bs = {-b.y, b.x};
    f32x2 r = a.xx * b;
    return __builtin_elementwise_fma(a.yy, bs, r);
  } else {
    float t; asm("v_mul_f32 %0, %1, %2" : "=v"(t) : "v"(a.x), "v"(b.x));
    return f32x2{__builtin_fmaf(-a.y, b.y, t), __builtin_fmaf(a.y, b.x, a.x * b.y)};
  }
}
DI f32x2 csq(f32x2 a) {
  float t; asm("v_mul_f32 %0, %1, %1" : "=v"(t) : "v"(a.x));
  return f32x2{__builtin_fmaf(-a.y, a.y, t), (a.x + a.x) * a.y};
}
DI u32 toh(f32x2 v) { u32 r; asm("v_cvt_pk_f16_f32 %0, %1, %2" : "=v"(r) : "v"(v.x), "v"(v.y)); return r; }
template <bool CONJ> DI void hmul2(u32& r0, u32 a0, u32 b0, u32& r1, u32 a1, u32 b1) {
  if constexpr (!CONJ)
    asm("v_pk_mul_f16 %0, %2, %3 op_sel_hi:[0,1]\n\tv_pk_mul_f16 %1, %4, %5 op_sel_hi:[0,1]\n\t"
        "v_pk_fma_f16 %0, %2, %3, %0 op_sel:[1,1,0] op_sel_hi:[1,0,1] neg_lo:[0,1,0]\n\tv_pk_fma_f16 %1, %4, %5, %1 op_sel:[1,1,0] op_sel_hi:[1,0,1] neg_lo:[0,1,0]"
        : "=&v"(r0), "=&v"(r1) : "v"(a0), "v"(b0), "v"(a1), "v"(b1));
  else
    asm("v_pk_mul_f16 %0, %2, %3 op_sel_hi:[0,1] neg_hi:[0,1]\n\tv_pk_mul_f16 %1, %4, %5 op_sel_hi:[0,1] neg_hi:[0,1]\n\t"
        "v_pk_fma_f16 %0, %2, %3, %0 op_sel:[1,1,0] op_sel_hi:[1,0,1]\n\tv_pk_fma_f16 %1, %4, %5, %1 op_sel:[1,1,0] op_sel_hi:[1,0,1]"
        : "=&v"(r0), "=&v"(r1) : "v"(a0), "v"(b0), "v"(a1), "v"(b1));
}
DI f32x4 mfh(u32x4 a, u32x4 b, f32x4 c) { return __builtin_amdgcn_mfma_f32_16x16x32_f16(__builtin_bit_cast(f16x8, a), __builtin_bit_cast(f16x8, b), c, 0, 0, 0); }
DI f32x2 cexp2(float lr, float th, float sgn) {
  float m = __builtin_amdgcn_exp2f(lr), f = __builtin_amdgcn_fractf(th);
  f32x2 r = {m * __builtin_amdgcn_cosf(f), sgn * m * __builtin_amdgcn_sinf(f)};
  return r;
}
template <int CTRL> DI float dpp_add(float v) {
  return v + __builtin_bit_cast(float, __builtin_amdgcn_update_dpp(0, __builtin_bit_cast(int, v), CTRL, 0xf, 0xf, true));
}
template <int CTRL> DI float dpp_max(float v) {
  return fmaxf(v, __builtin_bit_cast(float, __builtin_amdgcn_update_dpp(0, __builtin_bit_cast(int, v), CTRL, 0xf, 0xf, true)));
}
DI float wave_max(float v) {
  v = dpp_max<0x128>(v); v = dpp_max<0x124>(v); v = dpp_max<0x122>(v); v = dpp_max<0x121>(v);
  const float a = __builtin_bit_cast(float, __builtin_amdgcn_readlane(__builtin_bit_cast(int, v), 0)), b = __builtin_bit_cast(float, __builtin_amdgcn_readlane(__builtin_bit_cast(int, v), 16)),
              c = __builtin_bit_cast(float, __builtin_amdgcn_readlane(__builtin_bit_cast(int, v), 32)), d = __builtin_bit_cast(float, __builtin_amdgcn_readlane(__builtin_bit_cast(int, v), 48));
  return fmaxf(fmaxf(a, b), fmaxf(c, d));
}
DI float swap16_add(float v) { auto r = __builtin_amdgcn_permlane16_swap(__float_as_uint(v), __float_as_uint(v), false, false); return __uint_as_float(r[0]) + __uint_as_float(r[1]); }
DI float swap32_add(float v) { auto r = __builtin_amdgcn_permlane32_swap(__float_as_uint(v), __float_as_uint(v), false, false); return __uint_as_float(r[0]) + __uint_as_float(r[1]); }
DI float red32(float v) {
  v = dpp_add<0x128>(v); v = dpp_add<0x124>(v); v = dpp_add<0x122>(v); v = dpp_add<0x121>(v);
  return swap16_add(v);
}
DI u32x4 ldflags4(u32 off) { CFENCE(); u32x4 x = *(volatile LAS u32x4*)lp<u32x4>(off); CFENCE(); return x; }
DI u32 ldflag(u32 off) { CFENCE(); u32 x = *(volatile LAS u32*)lp<u32>(off); CFENCE(); return x; }
DI void chk4(u32x4 x, u32 off, u32 v) {
  if (rfl(min(min(x.x, x.y), min(x.z, x.w))) < v) {
#pragma nounroll
    for (u32 n = 0; lds_min4(off) < v && n < LSPIN_CAP; n++) { SPIN_NAP(); }
  }
  CFENCE();
}
DI void chk1(u32 x, u32 off, u32 v) {
  if (rfl(x) < v) {
#pragma nounroll
    for (u32 n = 0; rfl(ldflag(off)) < v && n < LSPIN_CAP; n++) { SPIN_NAP(); }
  }
  CFENCE();
}

template <int NR> DI void kgen(const u32 (&pw)[6], u32 ldt, u32 LB, u32 lane, f32x4 (&ka)[2][2]) {
  const u32 c16 = lane & 15, gam = lane >> 4;
  const u32 sw = opq(LB + lane * 4), sr = opq(LB + c16 * 288 + gam * 16), sr2 = opq(LB + (16 + min(c16, 4u)) * 288 + gam * 16);
  const u32 sqa = opq(LB + (21 + (c16 & 3)) * 288 + gam * 16), sqb = opq(LB + (c16 < 4 ? 21 : 24 + (c16 >> 2)) * 288 + gam * 16), sqc = opq(LB + 28 * 288 + gam * 16);
  *lp<u32>(sw + 21 * 288) = 0x3c00u;
  SB();
  const float dt = rbf(__builtin_amdgcn_exp2f(bflo(ldt) * LOG2E));
#pragma unroll
  for (int i = 0; i < 4; i++) ka[i >> 1][i & 1] = f32x4{0, 0, 0, 0};
  u32x4 qa[4], qb_[4], qc[4], pf[2][4];
  auto kq = [&]() {
    u32x4 q0[4], q1[4];
#pragma unroll
    for (int k = 0; k <= 4; k++) {
      u32 a0, a1, a2, a3, b0, b1_, b2, b3;
      if (k < 4) hmul2<false>(a0, qa[k].x, qb_[k].x, a1, qa[k].y, qb_[k].y);
      else asm volatile("s_nop 1" : "+v"(q1[3]));
      SB();
      if (k > 0) ka[0][0] = mfh(pf[0][k - 1], q0[k - 1], ka[0][0]);
      SB();
      if (k < 4) hmul2<false>(a2, qa[k].z, qb_[k].z, a3, qa[k].w, qb_[k].w);
      SB();
      if (k > 0) ka[1][0] = mfh(pf[1][k - 1], q0[k - 1], ka[1][0]);
      SB();
      if (k < 4) hmul2<false>(b0, a0, qc[k].x, b1_, a1, qc[k].y);
      SB();
      if (k > 0) ka[0][1] = mfh(pf[0][k - 1], q1[k - 1], ka[0][1]);
      SB();
      if (k < 4) hmul2<false>(b2, a2, qc[k].z, b3, a3, qc[k].w);
      SB();
      if (k > 0) ka[1][1] = mfh(pf[1][k - 1], q1[k - 1], ka[1][1]);
      SB();
      if (k < 4) { q0[k] = u32x4{a0, a1, a2, a3}; q1[k] = u32x4{b0, b1_, b2, b3}; }
    }
  };
  auto prm = [&](int r, int i) { return r == 0 ? bflo(pw[i]) : bfhi(pw[i]); };
  u32 wh[NR];
  float unscale;
  {
    f32x2 wv[NR];
    float mx = 0.f;
#pragma unroll
    for (int r = 0; r < NR; r++) {
      wv[r] = cmul<false>(f32x2{prm(r, 2), -prm(r, 3)}, f32x2{prm(r, 4), -prm(r, 5)});
      mx = fmaxf(mx, fmaxf(fabsf(wv[r].x), fabsf(wv[r].y)));
    }
    const int ex = __builtin_amdgcn_frexp_expf(wave_max(mx));
#pragma unroll
    for (int r = 0; r < NR; r++) wh[r] = toh(f32x2{__builtin_amdgcn_ldexpf(wv[r].x, 14 - ex), __builtin_amdgcn_ldexpf(wv[r].y, 14 - ex)});
    unscale = __builtin_amdgcn_ldexpf(dt * LN2, ex - 14);
  }
  sfor<NR>([&](auto R) {
    constexpr int r = R;
    const float lr = dt * prm(r, 0) * LOG2E, th = dt * prm(r, 1) * 0.15915494309189535f;
    u32 zh[11];
    {
      f32x2 za = cexp2(lr, th, 1.f), zb = cexp2(lr * 32.f, th * 32.f, 1.f);
      zh[0] = toh(za); zh[5] = toh(zb);
#pragma unroll
      for (int i = 1; i < 6; i++) { if (i < 5) { za = csq(za); zh[i] = toh(za); } zb = csq(zb); zh[5 + i] = toh(zb); }
    }
    u32 p[21], z24, z96;
    p[0] = wh[r];
    hmul2<false>(z24, zh[3], zh[4], z96, zh[5], zh[6]);
    *lp<u32>(sw + 22 * 288) = zh[3]; *lp<u32>(sw + 23 * 288) = zh[4]; *lp<u32>(sw + 24 * 288) = z24; *lp<u32>(sw + 25 * 288) = zh[5];
    *lp<u32>(sw + 26 * 288) = zh[6]; *lp<u32>(sw + 27 * 288) = z96; *lp<u32>(sw + 28 * 288) = zh[7];
    hmul2<true>(p[1], p[0], zh[0], p[2], p[0], zh[1]);
    hmul2<true>(p[4], p[0], zh[2], p[8], p[0], zh[8]);
    hmul2<true>(p[12], p[0], zh[9], p[16], p[0], zh[10]);
    hmul2<true>(p[3], p[1], zh[1], p[5], p[1], zh[2]);
    hmul2<true>(p[6], p[2], zh[2], p[9], p[2], zh[8]);
    hmul2<true>(p[10], p[4], zh[8], p[13], p[4], zh[9]);
    hmul2<true>(p[14], p[8], zh[9], p[17], p[8], zh[10]);
    hmul2<true>(p[18], p[12], zh[10], p[20], p[16], zh[10]);
    hmul2<true>(p[7], p[3], zh[2], p[11], p[6], zh[8]);
    hmul2<true>(p[15], p[10], zh[9], p[19], p[14], zh[10]);
    SB();
#pragma unroll
    for (int i = 0; i < 21; i++) *lp<u32>(sw + i * 288) = p[i];
    CFENCE();
    if (r > 0) { SB(); kq(); }
    SB();
#pragma unroll
    for (int k = 0; k < 4; k++) { qa[k] = *lp<u32x4>(sqa + k * 64); qb_[k] = *lp<u32x4>(sqb + k * 64); qc[k] = *lp<u32x4>(sqc + k * 64); pf[0][k] = *lp<u32x4>(sr + k * 64); pf[1][k] = *lp<u32x4>(sr2 + k * 64); }
    CFENCE();
    SB();
  });
  kq();
#pragma unroll
  for (int i = 0; i < 4; i++) ka[i >> 1][i & 1] *= unscale;
}

extern "C" __global__ void __launch_bounds__(512, 2)
s4mlp(const u16* __restrict__ x, const u16* __restrict__ Are, const u16* __restrict__ Aim, const u16* __restrict__ Bre,
      const u16* __restrict__ Bim, const u16* __restrict__ Cre, const u16* __restrict__ Cim, const u16* __restrict__ logdt,
      const u16* __restrict__ Dsk, const u16* __restrict__ w1, const u16* __restrict__ b1, const u16* __restrict__ w2,
      u16* __restrict__ out, char* __restrict__ wsp) {
  const int lane = threadIdx.x & 63;
  const u32 wave = rfl(threadIdx.x >> 6);
  const u32 w = blockIdx.x, g8 = w & 7, r5 = w >> 3;
  STAMPS_DECL;
  rsrc_t rc;
  {
    u64 pc; u32 back, len;
    asm volatile("s_getpc_b64 %0\n1:\n\ts_mov_b32 %1, 1b-s4mlp\n\ts_mov_b32 %2, s4mlp_end-s4mlp" : "=s"(pc), "=s"(back), "=s"(len));
    rc = mk((const char*)(pc - back), len);
  }
  asm volatile("s_cmp_lg_u32 %3, 0\n\ts_cbranch_scc1 2f\n\ts_mov_b32 m0, %0\n\ts_nop 0\n\tbuffer_load_dword %1, %2, 0 offen lds\n2:"
               ::"s"(L_DUMMY), "v"(((threadIdx.x << 3) + (r5 & 7)) << 7), "s"(rc), "s"(wave) : "scc");
  *lp<u32x2>(L_SY + lane * 8) = u32x2{0, 0};
  const u32 pm = (g8 >> 1) * 4 + (r5 >> 3), pn = (g8 & 1) * 8 + (r5 & 7);
  const u32 qm = g8 * 4 + (r5 >> 3), qn = r5 & 7;
  const u32 pair = (w >> 4) * 8 + g8, odd = r5 & 1;
  const u32 ch0 = pair * 5 + odd * 3, nch = 3 - odd;
  const u32 rot = ((r5 >> 3) ^ r5) & 1;
  constexpr u32 rot1 = 0;
  auto ck = [&](u32 j) { return j < 6 ? j + 6 * rot : j - 6 * rot; };
  const rsrc_t rws = mk(wsp, WS_USED);
  const rsrc_t rx = mk(x, 2048 * 1280);
  const u32 c16 = lane & 15, gam = lane >> 4, tq = (lane >> 2) & 3, tp = lane & 3;

  const u32 tm = pm * 2 + (pn >> 3), tn = pn & 7;
  u32 vx[4];
#pragma unroll
  for (int j = 0; j < 4; j++) vx[j] = (lane >> 3) * 1280 + (((lane & 7) ^ (4 * (j & 1) + (lane >> 4))) << 4) + j * 9216;
  const u32 ldx = RB + wave * 4096;
  const u32 soxr[2] = {(pm * 128 + wave * 32) * 1280 + rot1 * 640, (pm * 128 + wave * 32) * 1280 - rot1 * 640};
  auto pieceX = [&](int i, int s) {
    if (i == 0) dma(ldx + ((s + 2) % 3) * 16384, vx[0], rx, soxr[s >= 5] + s * 128);
    if (i == 1) dma_next<1024>(vx[1], rx, soxr[s >= 5] + s * 128);
    if (i == 2) dma_next<2048>(vx[2], rx, soxr[s >= 5] + s * 128);
    if (i == 3) dma_next<3072>(vx[3], rx, soxr[s >= 5] + s * 128);
  };
  const rsrc_t rw1 = mk(w1, 640 * 3072);
  u32 vw[3];
#pragma unroll
  for (int j = 0; j < 3; j++) vw[j] = (((64 * j + lane) * 171) >> 11) * 2880 + lane * 16;
  const u32 d = min(ch0 + (wave & 3), 639u);
  u32 pw[6], ldt2 = 0, dsk2 = 0;
  asm volatile("" : "+v"(vx[0]), "+v"(vx[1]), "+v"(vx[2]), "+v"(vx[3]), "+v"(vw[0]), "+v"(vw[1]), "+v"(vw[2]));
  asm volatile("s_waitcnt lgkmcnt(0)\n\ts_barrier" ::"s"(x), "s"(Are), "s"(Aim), "s"(Bre), "s"(Bim), "s"(Cre), "s"(Cim), "s"(logdt), "s"(Dsk), "s"(w1), "s"(b1), "s"(w2), "s"(out), "s"(wsp) : "memory");
  STAMPS_GO;
  STAMP(0);
  if ((wave & 3) < nch) {
    const u32 ob = d * 384, o = wave < 4 ? 256 + lane * 2 : lane * 4, od = (d >> 1) * 4;
    const char *p0 = (const char*)Are + ob, *p1 = (const char*)Aim + ob, *p2 = (const char*)Bre + ob, *p3 = (const char*)Bim + ob, *p4 = (const char*)Cre + ob, *p5 = (const char*)Cim + ob;
    asm volatile("global_load_dword %0, %2, %3\n\tglobal_load_dword %1, %2, %4" : "=&v"(ldt2), "=&v"(dsk2) : "v"(od), "s"(logdt), "s"(Dsk));
    if (wave < 4)
      asm volatile("global_load_ushort %2, %6, %9\n\tglobal_load_ushort %3, %6, %10\n\tglobal_load_ushort %4, %6, %11\n\tglobal_load_ushort %5, %6, %12\n\tglobal_load_ushort %0, %6, %7\n\tglobal_load_ushort %1, %6, %8"
                   : "=&v"(pw[0]), "=&v"(pw[1]), "=&v"(pw[2]), "=&v"(pw[3]), "=&v"(pw[4]), "=&v"(pw[5]) : "v"(o), "s"(p0), "s"(p1), "s"(p2), "s"(p3), "s"(p4), "s"(p5));
    else
      asm volatile("global_load_dword %2, %6, %9\n\tglobal_load_dword %3, %6, %10\n\tglobal_load_dword %4, %6, %11\n\tglobal_load_dword %5, %6, %12\n\tglobal_load_dword %0, %6, %7\n\tglobal_load_dword %1, %6, %8"
                   : "=&v"(pw[0]), "=&v"(pw[1]), "=&v"(pw[2]), "=&v"(pw[3]), "=&v"(pw[4]), "=&v"(pw[5]) : "v"(o), "s"(p0), "s"(p1), "s"(p2), "s"(p3), "s"(p4), "s"(p5));
  }
  __builtin_amdgcn_s_sleep(BULK_DELAY);
  if (wave < 4) {
    dma3(RB + 49152 + 2 * 12288 + wave * 3072, vw[0], vw[1], vw[2], rw1, pn * 192 + 5 * rot1 * (64 * 3072) + wave * 16 * 3072);
#pragma unroll
    for (int i = 0; i < 4; i++) pieceX(i, 0);
    dma_flag(SY_F0 + wave * 4, rc);
  }
  asm volatile("s_cmp_lt_u32 %8, 4\n\ts_cbranch_scc1 3f\n\ts_waitcnt vmcnt(0)\n3:\n\ts_waitcnt vmcnt(8)"
               : "+v"(pw[0]), "+v"(pw[1]), "+v"(pw[2]), "+v"(pw[3]), "+v"(pw[4]), "+v"(pw[5]), "+v"(ldt2), "+v"(dsk2) : "s"(wave) : "memory", "scc");
  const u32 ldt = ldt2 >> ((d & 1) * 16), dsk = dsk2 >> ((d & 1) * 16);
  asm volatile("" ::: "memory");
  const u32 wave2 = (u32)__builtin_amdgcn_readfirstlane(opq(wave));
  const u32 vr = (lane >> 3) * 1280, ve = (lane >> 1) * 1280 + (lane & 1) * 16, vl = lane * 16;
  auto tile = [&](u32 v0, u32 v1, u32 r64, u32 cn) {
    const u32 so = r64 * 64 * 1280;
    dma4(L_XS, v0, v1 + 9216, v0 + 2 * 9216, v1 + 3 * 9216, rx, so + cn * 128);
    dma4(L_XS + 4096, v0, v1 + 9216, v0 + 2 * 9216, v1 + 3 * 9216, rx, so + cn * 128 + 32 * 1280);
    dma(L_XS + 8192, ve, rx, so + 1024 + cn * 32);
    dma(L_XS + 9216, ve, rx, so + 1024 + cn * 32 + 32 * 1280);
  };
  if (wave2 < 4) {
    __builtin_amdgcn_s_setprio(2);
    const u32 sym = SY_M + wave * 4;
    if (wave == 3) {
      u32x4 xr[10];
#pragma unroll
      for (int i = 0; i < 10; i++) asm volatile("buffer_load_dwordx4 %0, %1, %2, %3 offen offset:%4" : "=v"(xr[i]) : "v"(vr + (lane & 7) * 16), "s"(rx), "s"((pm * 128 + (pn ^ 8) * 8) * 1280), "i"(i * 128));
#pragma unroll
      for (int i = 0; i < 4; i++) {
        dma3(RB + 49152 + 3 * 12288 + i * 3072, vw[0], vw[1], vw[2], rw1, pn * 192 + (1 + 5 * rot1) * (64 * 3072) + i * 16 * 3072);
        dma4(RB + i * 4096, vx[0], vx[1], vx[2], vx[3], rx, (pm * 128 + i * 32) * 1280 + rot1 * 640 + 128);
      }
      dma_flag(SY_F + 4, rc);
      {
        u32 vt[3];
#pragma unroll
        for (int j = 0; j < 3; j++) vt[j] = (((64 * j + lane) * 171) >> 11) * (32 * 3072 - 1536) + (64 * j + lane) * 128;
        asm volatile("s_mov_b32 m0, %0\n\ts_nop 0\n\tbuffer_load_dword %1, %4, %5 offen lds\n\tbuffer_load_dword %2, %4, %5 offen lds\n\tbuffer_load_dword %3, %4, %5 offen lds"
                     ::"s"(L_DUMMY), "v"(vt[0]), "v"(vt[1]), "v"(vt[2]), "s"(rw1), "s"((128 + r5) * 3072 + (g8 & 1) * 1536));
      }
      asm volatile("s_waitcnt vmcnt(" VMX ")" : "+v"(xr[0]), "+v"(xr[1]), "+v"(xr[2]), "+v"(xr[3]), "+v"(xr[4]), "+v"(xr[5]), "+v"(xr[6]), "+v"(xr[7]), "+v"(xr[8]), "+v"(xr[9])::"memory");
      float s0 = 0.f, s1 = 0.f, q0 = 0.f, q1 = 0.f;
#pragma unroll
      for (int i = 0; i < 10; i++)
        asm("v_dot2_f32_bf16 %0, %4, %8, %0\n\tv_dot2_f32_bf16 %2, %4, %4, %2\n\tv_dot2_f32_bf16 %1, %5, %8, %1\n\tv_dot2_f32_bf16 %3, %5, %5, %3\n\t"
            "v_dot2_f32_bf16 %0, %6, %8, %0\n\tv_dot2_f32_bf16 %2, %6, %6, %2\n\tv_dot2_f32_bf16 %1, %7, %8, %1\n\tv_dot2_f32_bf16 %3, %7, %7, %3"
            : "+v"(s0), "+v"(s1), "+v"(q0), "+v"(q1) : "v"(xr[i].x), "v"(xr[i].y), "v"(xr[i].z), "v"(xr[i].w), "v"(0x3f803f80u));
      asm volatile("s_nop 3" : "+v"(s0), "+v"(s1), "+v"(q0), "+v"(q1));
      float sm = s0 + s1, sq = q0 + q1;
      sm = dpp_add<0xb1>(sm); sq = dpp_add<0xb1>(sq); sm = dpp_add<0x4e>(sm); sq = dpp_add<0x4e>(sq); sm = dpp_add<0x141>(sm); sq = dpp_add<0x141>(sq);
      const float mean = sm * (1.f / 640.f);
      const float var = rbf(fmaxf(__builtin_fmaf(-mean, mean, sq * (1.f / 640.f)), 0.f));
      const u32 pk = pk2(mean, __builtin_amdgcn_sqrtf(rbf(var + 1e-5f)));
      *lp<u32>(SY_RS + (lane >> 3) * 4) = pk;
    } else if (wave < nch) {
      f32x4 ka[2][2];
      kgen<1>(pw, ldt, wave == 0 ? RB + 16384 : RB + 49152 + (wave - 1) * 12288, lane, ka);
#pragma unroll
      for (int i = 0; i < 4; i++) *lp<f32x4>(L_SSM + wave * SSM_SZ + 512 + i * 1024 + vl) = ka[i >> 1][i & 1];
    }
    lds_set(SY_XF + wave * 4, 1);
    STAMP(1);
    const u32 h = lane >> 5;
    u32 ax[4];
#pragma unroll
    for (int ks = 0; ks < 4; ks++) ax[ks] = opq(ldx + (lane & 31) * 128 + (((2 * ks + h) ^ ((lane >> 1) & 7)) << 4));
    const u32 aw = opq(RB + 49152 + (8 * h + tq) * 192 + (gam & 1) * 32 + tp * 8);
    f32x16 acc[3];
#pragma unroll
    for (int ft = 0; ft < 3; ft++)
#pragma unroll
      for (int i = 0; i < 16; i++) acc[ft][i] = 0.f;
    u32x4 Xf[2][4];
    u32x2 Wf[2][4][3][2];
    auto rd = [&](int s, int i) {
      const int ks = i / 7, r = i % 7;
      if (r == 6) Xf[s & 1][ks] = *lp<u32x4>(ax[ks] + ((s + 2) % 3) * 16384);
      else Wf[s & 1][ks][r >> 1][r & 1] = trd(aw + ((s + 2) % 4) * 12288 + (ks * 16 + (r & 1) * 4) * 192 + (r >> 1) * 64);
    };
    auto mm1 = [&](int s, int i) {
      const int ks = s < 9 ? i / 3 : i % 4, ft = s < 9 ? i % 3 : i / 4;
      acc[ft] = mf32(frag(Wf[s & 1][ks][ft][0], Wf[s & 1][ks][ft][1]), fragv(Xf[s & 1][ks]), acc[ft]);
    };
    chk4(ldflags4(SY_F0), SY_F0, 1);
    chk1(ldflag(SY_XF), SY_XF, 1);
    STAMP(2);
#pragma unroll
    for (int i = 0; i < 28; i++) { rd(0, i); if (i % 7 == 6) pieceX(i / 7, 2); }
    u32 fl = ldflag(SY_F + 4);
    lds_set(sym, 1);
    __builtin_amdgcn_s_setprio(M_PRIO);
    const rsrc_t rw2 = mk(w2, 1536 * 1280);
    const u32 v2 = (lane >> 3) * 1280 + (((lane & 7) ^ (((lane >> 4) & 3) << 1)) << 4);
    const u32 so2b = ((wave >> 1) * 768 + (wave & 1) * 32) * 1280 + qn * 128;
    const u32 so2x = WS_W2X + qn * 49152 + ((wave >> 1) * 768 + (wave & 1) * 32) * 32;
    f32x4 cs[12], bb[12];
    f32x2 st;
    sfor<10>([&](auto S_) {
      constexpr int s = S_;
      if (s == 9) STAMP(4);
      if (s == 4) STAMP(11);
      chk1(fl, s < 9 ? SY_F + (s + 1) * 4 : SY_EPI1, 1);
      if (s > 0 && s < 9) VMCNT(4);
      SB();
#pragma unroll
      for (int gi = 0; gi < 12; gi++) {
        mm1(s, gi);
        if (gi < 8) {
          if (s < 9) {
#pragma unroll
            for (int i = (gi * 7) / 2; i < ((gi + 1) * 7) / 2; i++) rd(s + 1, i);
          } else {
            const u32 cb = opq(L_CS + h * 16);
            if (gi == 0) st = *lp<f32x2>(L_ST + (wave * 32 + (lane & 31)) * 8);
#pragma unroll
            for (int i = gi * 3; i < gi * 3 + 3; i++) { if (i & 1) bb[i >> 1] = *lp<f32x4>(cb + 384 + (i >> 1) * 32); else cs[i >> 1] = *lp<f32x4>(cb + (i >> 1) * 32); }
          }
        }
        if (gi < 4) {
          if (s < 7) pieceX(gi, s + 3);
          else dma(ldx + (s - 7) * 16384 + gi * 1024, v2, rw2, so2b + (ck(s - 7) * 64 + gi * 8) * 1280);
        }
        if (gi == 9 && s == 9) dma_flag(SY_F + 64 + wave * 4, rc);
        if (gi == 7 && s < 9) lds_set(sym, s + 2);
        if (gi == 9 && s < 9) fl = ldflag(s < 8 ? SY_F + (s + 2) * 4 : SY_EPI1);
        SB();
      }
    });
    __builtin_amdgcn_s_setprio(2);
    STAMP(3);
    {
      const u32 vo = WS_ACT + ((pm * 4 + wave) * 48 + pn * 3) * 2048 + lane * 16;
#pragma unroll
      for (int it = 0; it < 6; it++) {
        const int ft = it >> 1, eta = it & 1;
        u32x4 o;
#pragma unroll
        for (int p = 0; p < 4; p++) {
          const int ci = ft * 4 + eta * 2 + (p >> 1), j = (p & 1) * 2;
          const f32x2 two = {2.f, 2.f}, one = {1.f, 1.f};
          f32x2 hh = __builtin_elementwise_fma(st.xx, f32x2{acc[ft][eta * 8 + 2 * p], acc[ft][eta * 8 + 2 * p + 1]},
                                               __builtin_elementwise_fma(st.yy, f32x2{cs[ci][j], cs[ci][j + 1]}, f32x2{bb[ci][j], bb[ci][j + 1]}));
          f32x2 e = {__builtin_amdgcn_exp2f(hh.x), __builtin_amdgcn_exp2f(hh.y)};
          f32x2 dd = __builtin_elementwise_fma(e, e + two, two);
          f32x2 rr = {__builtin_amdgcn_rcpf(dd.x), __builtin_amdgcn_rcpf(dd.y)};
          f32x2 ac = hh * __builtin_elementwise_fma(-two, rr, one);
          o[p] = pk2(ac.x, ac.y);
        }
        st16c(o, rws, vo, it * 1024);
      }
    }
    lds_set(sym, 13);
    const u32x2 tag = *lp<u32x2>(SY_TAG);
    STAMP(5);
    __builtin_amdgcn_s_setprio(3);
    const u32 m2 = wave & 1, kh = wave >> 1;
    const u32 eta2 = gam >> 1, h2 = gam & 1;
    u32 bw[4], bx;
    {
      const u32 row = eta2 * 16 + h2 * 4 + tq, sg = (2 * h2 + (tq >> 1)) & 3;
#pragma unroll
      for (int nt = 0; nt < 4; nt++) bw[nt] = opq(RB + kh * 8192 + row * 128 + ((nt ^ sg) << 5) + tp * 8);
      bx = opq(RB + 81920 + kh * 2048 + row * 32 + tp * 8);
    }
    u32x2 W2[24][5][2];
    u32 bt[5];
    auto rd2 = [&](int k, int i) {
      const int c = k >> 1, sl = c < 5 ? c : c < 8 ? c - 5 : c - 7, kk = k & 1, nt = i >> 1, t = i & 1;
      const u32 in = nt < 4 ? kk * 4096 + t * 1024 : kk * 1024 + t * 256;
      if (c == 8 || c == 9) {
        if (k == 16 && t == 0) bt[nt] = opq(nt < 4 ? bw[nt] - RB : bx - RB - 81920 + 32768);
        W2[k][nt][t] = trd(bt[nt] + (c - 8) * (nt < 4 ? 16384 : 4096) + in);
      } else if (sl == 4 && nt < 4) {
        if (kk == 0 && t == 0) bt[nt] = opq(bw[nt] + 65536);
        W2[k][nt][t] = trd(bt[nt] + in);
      } else W2[k][nt][t] = trd(nt < 4 ? bw[nt] + sl * 16384 + in : bx + sl * 4096 + in);
    };
    chk4(ldflags4(SY_F + 64), SY_F + 64, 1);
    chk1(ldflag(SY_F + 44), SY_F + 44, 1);
#pragma unroll
    for (int i = 0; i < 60; i++) rd2(i / 10, i % 10);
    lds_set(sym, 14);
    publish<1>(rws, WS_FB + ((pm * 4 + wave) * 16 + pn) * 64, tag, lane);
    STAMP(6);
    __builtin_amdgcn_s_setprio(0);
    const u32 rl = m2 * 32 + kh * 16;
#ifdef M_POLL
    { u32 po[1] = {lane < 8 ? lane * 64 : OOR}; poll_flags<1, B_SLEEP>(rws, po, WS_FB + ((qm * 2 + m2) * 16 + kh * 8) * 64, tag); }
#else
    chk1(0, SY_B + (m2 * 2 + kh) * 4, 1);
#endif
    STAMP(7);
    const u32 va = eta2 * 1024 + (h2 * 32 + c16) * 16 + kh * 256, vb = va ^ 256;
    const u32 soa0 = WS_ACT + ((qm * 2 + m2) * 48 + kh * 24) * 2048;
    const u32 soa = soa0 + rot * 12 * 2048, soa2 = soa0 - rot * 12 * 2048;
    constexpr int WIN = G2_WIN;
    u32x4 af[32][2];
#pragma unroll
    for (int k = 0; k < WIN; k++) { af[k][0] = ld16c(rws, va, soa + k * 2048); af[k][1] = ld16c(rws, vb, soa + k * 2048); }
    f32x4 c2[5][2];
#pragma unroll
    for (int nt = 0; nt < 5; nt++) { c2[nt][0] = f32x4{0, 0, 0, 0}; c2[nt][1] = f32x4{0, 0, 0, 0}; }
    SB();
    u32 ff = 0;
    u32 e_xo, e_xe, e_rb, e_rp;
    u32x2 sv[5], xv[5];
    float sf[20], xf[20];
    sfor<24>([&](auto K_) {
      constexpr int k = K_;
      constexpr int kr = k < 4 ? k + 6 : k >= 8 && k < 22 ? k + 2 : -1;
      if (k == 7 || k == 13 || k == 17) ff = ldflag(k == 7 ? SY_F + 48 : k == 13 ? SY_FULL : SY_F + 52);
      if (k == 8 || k == 14 || k == 18) chk1(ff, k == 8 ? SY_F + 48 : k == 14 ? SY_FULL : SY_F + 52, 1);
      if (k == 21) ff = ldflag(SY_EPI2);
      if (k == 22) {
        STAMP(8);
        chk1(ff, SY_EPI2, 1);
        STAMP(9);
        const u32 ln = __builtin_amdgcn_mbcnt_hi(~0u, __builtin_amdgcn_mbcnt_lo(~0u, opq(0u)));
        const u32 c16 = ln & 15, gam = ln >> 4, tq = (ln >> 2) & 3;
        const u32 e_sb = opq(L_SIMG + (gam * 4 + tq) * 128 + ((((rl >> 4) ^ (tq >> 1) ^ ((gam & 1) << 1)) & 3) << 5) + (ln & 3) * 8);
        e_xo = L_XS + (rl + c16) * 128 + (((gam >> 1) ^ (c16 & 7)) << 4) + (gam & 1) * 8;
        e_xe = L_XS + 8192 + (rl + c16) * 32 + gam * 8;
        e_rb = opq(L_RED + kh * 16384 + m2 * 5120 + ln * 16); e_rp = opq(L_RED + (kh ^ 1) * 16384 + m2 * 5120 + ln * 16);
#pragma unroll
        for (int nt = 0; nt < 5; nt++) { sv[nt] = trd(e_sb + nt * 2048); xv[nt] = *lp<u32x2>(nt < 4 ? ((e_xo - L_XS) ^ (nt * 32)) + L_XS : e_xe); }
      }
      SB();
#pragma unroll
      for (int j = 0; j < 10; j++) {
        const int i = k < 23 ? j : j < 5 ? 2 * j + 1 : 2 * (j - 5);
        c2[i >> 1][i & 1] = mf16(frag(W2[k][i >> 1][0], W2[k][i >> 1][1]), fragv(af[k][i & 1]), c2[i >> 1][i & 1]);
        if (kr >= 0) { if (k >= 8) rd2(kr, i); else if (i >= 2) { rd2(kr, i - 2); if (i >= 8) rd2(kr, i); } }
        if (k >= 1 && k - 1 + WIN < 24 && (i == 0 || i == 5)) af[k - 1 + WIN][i / 5] = ld16c(rws, i ? vb : va, (k - 1 + WIN < 12 ? soa : soa2) + (k - 1 + WIN) * 2048);
        if (k == 23 && j >= 5) *lp<f32x4>(e_rb + (j - 5) * 1024) = c2[j - 5][1];
        SB();
      }
      if (k == 3) lds_set(sym, 16);
      if (k == 9) lds_set(sym, 17);
      if (k == 13) lds_set(sym, 18);
    });
    lds_set(SY_RED + wave * 4, 1);
#pragma unroll
    for (int i = 0; i < 10; i++) {
      sf[2 * i] = bflo(sv[i >> 1][i & 1]); sf[2 * i + 1] = bfhi(sv[i >> 1][i & 1]); xf[2 * i] = bflo(xv[i >> 1][i & 1]); xf[2 * i + 1] = bfhi(xv[i >> 1][i & 1]);
      asm volatile("" : "+v"(sf[2 * i]), "+v"(sf[2 * i + 1]), "+v"(xf[2 * i]), "+v"(xf[2 * i + 1]));
      if (i == 3) ff = ldflag(SY_RED + (wave ^ 2) * 4);
    }
    chk1(ff, SY_RED + (wave ^ 2) * 4, 1);
    {
      f32x4 pv[5];
#pragma unroll
      for (int nt = 0; nt < 5; nt++) pv[nt] = *lp<f32x4>(e_rp + nt * 1024);
      const u32 ln = __builtin_amdgcn_mbcnt_hi(~0u, __builtin_amdgcn_mbcnt_lo(~0u, opq(0u)));
      const u32 e_ob = opq(L_XS + (rl + (ln >> 3)) * 128 + (((ln & 7) ^ (ln >> 3)) << 4)), e_oe = opq(L_XS + 8192 + rl * 32 + (ln & 31) * 16);
      const u32 e_vo = opq((qm * 64 + rl + (ln >> 3)) * 1280 + (ln & 7) * 16 + qn * 128), e_vo2 = opq(e_vo + 8 * 1280);
      const u32 e_ve = opq(ln < 32 ? (qm * 64 + rl + (ln >> 1)) * 1280 + (ln & 1) * 16 + 1024 + qn * 32 : OOR);
      SB();
#pragma unroll
      for (int nt = 0; nt < 5; nt++) {
        u32x2 r;
#pragma unroll
        for (int j = 0; j < 2; j++) {
          const int e = nt * 4 + j * 2;
          const f32x2 o = f32x2{pv[nt][2 * j], pv[nt][2 * j + 1]} + f32x2{c2[nt][0][2 * j], c2[nt][0][2 * j + 1]};
          const f32x2 dn = f32x2{__builtin_amdgcn_exp2f(-o.x), __builtin_amdgcn_exp2f(-o.y)} + f32x2{1.f, 1.f};
          const f32x2 rc_ = {__builtin_amdgcn_rcpf(dn.x), __builtin_amdgcn_rcpf(dn.y)};
          const f32x2 y = __builtin_elementwise_fma(o * f32x2{sf[e], sf[e + 1]}, rc_, f32x2{xf[e], xf[e + 1]});
          r[j] = pk2(y.x, y.y);
        }
        *lp<u32x2>(nt < 4 ? ((e_xo - L_XS) ^ (nt * 32)) + L_XS : e_xe) = r;
      }
      CFENCE();
      const rsrc_t ro = mk(out, 2048 * 1280);
      const u32x4 v0 = *lp<u32x4>(e_ob), v1 = *lp<u32x4>(e_ob + 1024), v2_ = *lp<u32x4>(e_oe);
      st16(v0, ro, e_vo, 0);
      st16(v1, ro, e_vo2, 0);
      st16(v2_, ro, e_ve, 0);
    }
    STAMP(10);
  } else if (wave2 == 7) {
    __builtin_amdgcn_s_setprio(3);
    const rsrc_t rw2 = mk(w2, 1536 * 1280);
    u32 v2[4];
    auto ldW = [&](u32 s) {
      const u32 so = pn * 192 + (s < 5 ? s + 5 * rot1 : s - 5 * rot1) * (64 * 3072), ld = RB + 49152 + ((s + 2) & 3) * 12288;
#pragma unroll
      for (int i = 0; i < 4; i++) dma3(ld + i * 3072, vw[0], vw[1], vw[2], rw1, so + i * 16 * 3072);
      dma_flag(SY_F + s * 4, rc);
    };
    auto ld2q = [&](u32 j, u32 slot, u32 q) {
      dma4(RB + slot * 16384 + q * 4096, v2[0], v2[1], v2[2], v2[3], rw2, ((q >> 1) * 768 + ck(j) * 64 + (q & 1) * 32) * 1280 + qn * 128);
    };
    auto ld2x = [&](u32 j, u32 slot) {
      dma2c(RB + 81920 + slot * 4096, vl, rws, WS_W2X + qn * 49152 + ck(j) * 2048);
      dma2c(RB + 81920 + slot * 4096 + 2048, vl, rws, WS_W2X + qn * 49152 + 768 * 32 + ck(j) * 2048);
    };
    { const u32 vt = vr + (((lane & 7) ^ (((lane >> 4) & 1) << 1)) << 4); tile(vt, vt ^ 64, tm, tn); }
    const u32x2 ep = ld8c(rws, 0, WS_EPOCH);
    SB();
    VMCNT(1);
    STAMP(9);
    {
      u32x4 t[10];
      const u32 sg = (tq >> 1) | ((gam & 1) << 1);
      const u32 ra = L_XS + (gam * 8 + tq) * 128 + tp * 8, re = opq(L_XS + 8192 + (gam * 8 + tq) * 32 + tp * 8);
#pragma unroll
      for (int cb = 0; cb < 5; cb++) {
        const u32 a = cb < 4 ? opq(ra + ((cb ^ sg) << 5)) : re;
#pragma unroll
        for (int hf = 0; hf < 2; hf++) {
          u32x2 a0 = trd(a + hf * (cb < 4 ? 4096 : 1024)), a1 = trd(a + hf * (cb < 4 ? 4096 : 1024) + (cb < 4 ? 512 : 128));
          t[cb * 2 + hf] = u32x4{a0.x, a0.y, a1.x, a1.y};
        }
      }
      CFENCE(); SB();
      const u32 wa0 = opq(L_XS + c16 * 128 + ((gam ^ (c16 & 7)) << 4)), wa1 = opq(wa0 ^ 64);
#pragma unroll
      for (int cb = 0; cb < 5; cb++) { *lp<u32x4>(wa0 + cb * 2048) = t[cb * 2]; *lp<u32x4>(wa1 + cb * 2048) = t[cb * 2 + 1]; }
      *lp<u32x4>(SY_TAG) = u32x4{ep.x + 1, ep.y + 0x9e3779b9u, 1u, 0u};
      CFENCE(); SB();
      const u32 rb = opq(L_XS + (lane >> 3) * 128 + (((lane & 7) ^ (lane >> 3)) << 4));
#pragma unroll
      for (int i = 0; i < 10; i++) t[i] = *lp<u32x4>(rb + i * 1024);
      SB();
      const u32 vo = (lane >> 3) * 4096 + (lane & 7) * 16 + WS_XT + tm * 128;
#pragma unroll
      for (int i = 0; i < 10; i++) st16c(t[i], rws, vo, (i < 8 ? tn * 64 + i * 8 : 512 + tn * 16 + (i - 8) * 8) * 4096);
      SB();
    }
    STAMP(10);
    lds_spin4(SY_XF, 1);
    st16c(*lp<u32x4>(SY_RS + (lane & 1) * 16), rws, lane < 2 ? WS_STG + lane * 4096 + (pm * 16 + (pn ^ 8)) * 16 : OOR, 0);
    SB();
    ldW(2); ldW(3);
    VMCNT(26);
    st8c(u32x2{ep.x + 1, ep.y + 0x9e3779b9u}, rws, lane == 0 ? 0 : OOR, WS_F1 + g8 * REP + r5 * 64);
    STAMP(1);
#pragma unroll
    for (int j = 0; j < 4; j++) v2[j] = vr + (((lane & 7) ^ (((lane >> 4) & 3) << 1)) << 4) + j * 9216;
#pragma unroll 1
    for (u32 s = 4; s < 10; s++) { lds_spin4(SY_M, s - 3); ldW(s); }
    STAMP(5);
    {
      const u32x2 tag = get_tag();
      lds_wait(SY_W2X, 1);
      STAMP(8);
      u32x4 g[5];
      const u32x2 ps = ld8c(rws, ((lane >> 1) & 1) * 4096 + (lane >> 2) * 16 + (lane & 1) * 8, WS_STG + pm * 256);
#pragma unroll
      for (int k = 0; k < 5; k++) g[k] = ld16c(rws, min(lane, 23) * 16, WS_CSP + k * 6144 + pn * 384);
      const u32x2 bv = *(const u32x2*)(b1 + pn * 96 + min(lane, 23) * 4);
      if (w == 0) st8c(tag, rws, lane == 0 ? 0 : OOR, WS_EPOCH);
      const float r0 = LOG2E * __builtin_amdgcn_rcpf(bfhi(ps.x)), r1 = LOG2E * __builtin_amdgcn_rcpf(bfhi(ps.y));
      *lp<f32x4>(L_ST + lane * 16) = f32x4{r0, -bflo(ps.x) * r0, r1, -bflo(ps.y) * r1};
      f32x4 cs = __builtin_bit_cast(f32x4, g[0]);
#pragma unroll
      for (int k = 1; k < 5; k++) cs += __builtin_bit_cast(f32x4, g[k]);
      if (lane < 24) {
        *lp<f32x4>(L_CS + lane * 16) = cs;
        *lp<f32x4>(L_B1 + lane * 16) = f32x4{bflo(bv.x) * LOG2E, bfhi(bv.x) * LOG2E, bflo(bv.y) * LOG2E, bfhi(bv.y) * LOG2E};
      }
      lds_set(SY_EPI1, 1);
    }
    { const u32 vs = vr + (((lane & 7) ^ (lane >> 3)) << 4); tile(vs, vs, qm, qn); }
    STAMP(2);
    lds_spin4(SY_M, 7);
    ld2q(3, 3, 0); ld2q(3, 3, 1); ld2q(3, 3, 2);
    lds_spin4(SY_M, 8);
    ld2q(3, 3, 3); ld2q(4, 4, 0); ld2q(4, 4, 1);
    lds_spin4(SY_M, 9);
    ld2q(4, 4, 2); ld2q(4, 4, 3);
    lds_spin4(SY_M, 10);
#pragma unroll 1
    for (u32 j = 0; j < 5; j++) ld2x(j, j);
    dma_flag(SY_F + 44, rc);
    STAMP(3);
    u32x4 pk_[2][20];
#pragma unroll
    for (int j = 0; j < 2; j++)
#pragma unroll
      for (int i = 0; i < 20; i++) {
        if (i < 16) pk_[j][i] = ld16(rw2, v2[0], ((i >> 3) * 768 + ck(8 + j) * 64 + (i & 7) * 8) * 1280 + qn * 128);
        else pk_[j][i] = ld16c(rws, vl, WS_W2X + qn * 49152 + (((i - 16) >> 1) * 768 + ck(8 + j) * 64) * 32 + (i & 1) * 1024);
      }
    SB();
    STAMP(4);
#ifndef M_POLL
    {
      const u32x2 tag = *lp<u32x2>(SY_TAG);
      lds_spin4(SY_M, 14);
#pragma unroll 1
      for (u32 j = 0; j < 3; j++) {
#pragma unroll
        for (int q = 0; q < 4; q++) ld2q(5 + j, j, q);
        ld2x(5 + j, j);
      }
      dma_flag(SY_F + 48, rc);
      u32 po = (lane >> 1) * 64;
      const u32 oa = (lane >> 5) * 48 * 2048 + ((lane >> 1) & 15) * 6144 + (qn * 2 + (lane & 1)) * 128, sa = WS_ACT + qm * 96 * 2048;
#pragma nounroll
      for (u32 spin = 0; spin < SPIN_CAP; spin++) {
        CFENCE();
        const u32x2 v = ld8c(rws, po, WS_FB + qm * 32 * 64);
        const bool nw = v.x == tag.x && v.y == tag.y;
        if (nw) po = OOR;
        const u64 pend = __builtin_amdgcn_ballot_w64(po != OOR);
        *lp<u32>(SY_B + (lane & 3) * 4) = ((pend >> ((lane & 3) * 16)) & 0xffff) == 0;
        if (__builtin_amdgcn_ballot_w64(nw) != 0)
          asm volatile("s_mov_b32 m0, %0\n\ts_nop 0\n\tbuffer_load_dword %1, %2, %3 offen sc1 lds\n\tbuffer_load_dword %1, %2, %4 offen sc1 lds\n\tbuffer_load_dword %1, %2, %5 offen sc1 lds"
                       ::"s"(L_DUMMY), "v"(nw ? oa : OOR), "s"(rws), "s"(sa), "s"(sa + 2048), "s"(sa + 4096));
        if (pend == 0) break;
        __builtin_amdgcn_s_sleep(B_SLEEP);
      }
      *lp<u32x4>(SY_B) = u32x4{1u, 1u, 1u, 1u};
      CFENCE();
    }
    STAMP(11);
#endif
    lds_wait4(SY_SDONE, 1);
    const u32 wb = opq(L_SSM + vl);
#pragma unroll
    for (int j = 0; j < 2; j++)
#pragma unroll
      for (int i = 0; i < 20; i++) *lp<u32x4>(wb + (i < 16 ? j * 16384 + i * 1024 : 32768 + j * 4096 + (i - 16) * 1024)) = pk_[j][i];
    lds_set(SY_FULL, 1);
    STAMP(6);
    lds_wait4(SY_M, 16);
#pragma unroll 1
    for (u32 j = 0; j < 2; j++) {
#pragma unroll
      for (int q = 0; q < 4; q++) ld2q(10 + j, 3 + j, q);
      ld2x(10 + j, 3 + j);
    }
    dma_flag(SY_F + 52, rc);
    VMCNT(0);
    STAMP(7);
  } else if (wave2 - 4 < nch) {
    const u32 LU = L_SSM + (wave - 4) * SSM_SZ, LB = LU + 4608;
    __builtin_amdgcn_s_setprio(S_PRIO);
    STAMP(1);
    f32x4 ka[2][2];
    kgen<2>(pw, ldt, LB, lane, ka);
    STAMP(2);
    const u32 fk = ldflag(SY_XF + (wave - 4) * 4);
    u32x4 tb[8];
    {
      const u32 dum = LB + LB_DUM + lane * 8;
      const bool g2 = gam == 2, g3 = gam == 3, lo3 = g3 && c16 < 2;
      const u32 kb = LB + 32 + 16 * c16 + (gam < 2 ? 8 * gam : g2 ? 512 : 1024), t4 = LB + T4 + 116 + 4 * c16;
      tb[4] = u32x4{(!g3 || lo3) ? kb : dum, (g2 || lo3) ? kb + 8 : g3 ? t4 : dum, g2 ? kb + 12 : g3 ? t4 + 128 : dum, ((g2 && c16 > 0) || lo3) ? kb - 2 : dum};
      tb[5] = u32x4{!g3 ? kb + 256 : dum, g2 ? kb + 264 : g3 ? t4 + 64 : dum, g2 ? kb + 268 : g3 ? t4 + 192 : dum, g2 ? kb + 254 : dum};
      tb[6] = u32x4{gam < 2 ? 0x03020100u : 0x01000100u, gam < 2 ? 0x07060504u : g2 ? 0x05040302u : 0x03020100u,
                    g2 ? 0x05040504u : c16 < 2 ? 0x03020302u : 0x03020100u, g2 ? 0x07060706u : 0x07060504u};
      tb[7] = u32x4{g2 ? 0x05040504u : 0x03020100u,
                    lane == 0 ? LB + (16 + 518) * 2 : lane == 1 ? LB + T4 + 186 * 2 : lane < 11 ? LB + (16 + 577 + lane) * 2 : lane < 13 ? LB + T8 + (18 + lane) * 2 : dum,
                    0u, LB + lane * 16 + (lane < 2 ? 0 : 1024)};
      asm volatile("" : "+v"(tb[4]), "+v"(tb[5]), "+v"(tb[6]), "+v"(tb[7]));
      SB();
    }
    if (lane < 32) *lp<u32x4>(LU + lane * 16) = u32x4{0, 0, 0, 0};
    const u32 lb16 = opq(LB + lane * 16);
    *lp<u32x4>(tb[7].w) = u32x4{0, 0, 0, 0};
    *lp<u32x4>(lb16 + T4) = u32x4{0, 0, 0, 0};
    CFENCE();
    chk1(fk, SY_XF + (wave - 4) * 4, 1);
#pragma unroll
    for (int i = 0; i < 4; i++) ka[i >> 1][i & 1] += *lp<f32x4>(LU + 512 + i * 1024 + vl);
    lds_set(SY_KD + (wave - 4) * 4, 1);
    if (wave == 4) lds_set(SY_KD + 12, 1);
    if (lane == 0) ka[0][0][0] += bflo(dsk) * LN2;
    {
      u32 a01[2], a23[2], b01[2], b23[2];
#pragma unroll
      for (int qb = 0; qb < 2; qb++) {
        a01[qb] = pk2(ka[0][qb][0], ka[0][qb][1]); a23[qb] = pk2(ka[0][qb][2], ka[0][qb][3]);
        b01[qb] = pk2(ka[1][qb][0], ka[1][qb][1]); b23[qb] = pk2(ka[1][qb][2], ka[1][qb][3]);
      }
      const u32 t8a = opq(LB + T8 + (29 + c16 + 128 * gam) * 2);
#pragma unroll
      for (int qb = 0; qb < 2; qb++) {
        *lp<u32x2>(tb[4 + qb].x) = u32x2{__builtin_amdgcn_perm(a23[qb], a01[qb], tb[6].x), __builtin_amdgcn_perm(a23[qb], a01[qb], tb[6].y)};
        *lp<u32>(tb[4 + qb].y) = __builtin_amdgcn_perm(a23[qb], a01[qb], qb ? tb[7].x : tb[6].z);
        *lp<u32>(tb[4 + qb].z) = __builtin_amdgcn_perm(a23[qb], a01[qb], tb[6].w);
        *lp<u16>(t8a + qb * 32) = (u16)b01[qb];
        *lp<u16>(t8a + qb * 32 + 64) = (u16)(b01[qb] >> 16);
        *lp<u16>(t8a + qb * 32 + 128) = (u16)b23[qb];
        *lp<u16>(t8a + qb * 32 + 192) = (u16)(b23[qb] >> 16);
      }
      CFENCE();
      *lp<u16>(tb[4].w) = (u16)a01[0];
      *lp<u16>(tb[5].w) = (u16)a01[1];
      const u32 k520 = __builtin_amdgcn_readlane(a01[0], 49), k1024 = __builtin_amdgcn_readlane(b01[0], 0), k1032 = __builtin_amdgcn_readlane(b01[0], 1);
      *lp<u16>(tb[7].y) = (u16)(lane == 0 ? k520 : lane < 4 ? k1024 : lane < 11 ? k1032 : 0u);
    }
    CFENCE();
    {
      const u32 lb2 = opq(LB + (64 + min((u32)lane, 13u)) * 16);
      u32x4 wv[2]; u32x2 yv[2];
      wv[0] = *lp<u32x4>(lb16); yv[0] = *lp<u32x2>(lb16 - 8); wv[1] = *lp<u32x4>(lb2); yv[1] = *lp<u32x2>(lb2 - 8);
      SB();
#pragma unroll
      for (int ps = 0; ps < 2; ps++) {
        const u32 b = ps ? lb2 : lb16;
        const u32x4 a = wv[ps]; const u32x2 y = yv[ps];
        const u32 s0 = __builtin_amdgcn_alignbit(y.y, y.x, 16), s1 = __builtin_amdgcn_alignbit(a.x, y.y, 16), s2 = __builtin_amdgcn_alignbit(a.y, a.x, 16),
                  s3 = __builtin_amdgcn_alignbit(a.z, a.y, 16), s4 = __builtin_amdgcn_alignbit(a.w, a.z, 16);
        *lp<u32x4>(b + 1344) = u32x4{s1, s2, s3, s4};
        *lp<u32x4>(b + 2688) = u32x4{y.y, a.x, a.y, a.z};
        *lp<u32x4>(b + 4032) = u32x4{s0, s1, s2, s3};
      }
      CFENCE();
    }
    bf16x8 Ff[18], H4[6], H8[4];
    {
      const u32 ha = opq(LB + T4 + (4 * c16 + 8 * gam) * 2), ha2 = opq(ha + 8);
#pragma unroll
      for (int e = 0; e < 6; e++) H4[e] = frag(*(volatile LAS u32x2*)lp<u32x2>(ha + e * 64), *(volatile LAS u32x2*)lp<u32x2>(ha2 + e * 64));
      const u32 hb = opq(LB + T8 + (2 * c16 + 8 * gam) * 2);
#pragma unroll
      for (int e = 0; e < 4; e++) {
        u32x4 v;
#pragma unroll
        for (int j = 0; j < 4; j++) v[j] = *(volatile LAS u32*)lp<u32>(hb + e * 64 + j * 4);
        H8[e] = fragv(v);
      }
      const u32 fa = opq(LB + tq * 1344 + (28 - 8 * gam + 4 * tp) * 2);
#pragma unroll
      for (int m = 0; m < 18; m++) { const int mm = m < 17 ? m : 18; Ff[m] = frag(trd(fa + mm * 64 + 8), trd(fa + mm * 64)); }
    }
    CFENCE();
    *lp<u32x4>(LB + (lane == 0 ? 0 : lane < 9 ? S5 + 3072 - 16 : lane < 16 ? S3 + 3072 - 144 : lane < 18 ? S9 + 2048 - 256 : S7 + 2048 - 288) + min(lane, 19) * 16) = u32x4{0, 0, 0, 0};
#pragma unroll
    for (int wd = 0; wd < 4; wd++) {
      const u32 msk = ((((1u << (2 * wd + 3)) - 1u) << (c16 + 7 - wd)) >> (8 * gam)) & 255u, m2 = msk | (msk << 15);
#pragma unroll
      for (int j = 0; j < 4; j++) tb[wd][j] = __umul24((m2 >> (2 * j)) & 0x10001u, 0x3f80u);
      asm volatile("" : "+v"(tb[wd]));
    }
    STAMP(3);
    if (wave < 6) {
      u32x4 sa[2][2], xa[3][2];
      if (wave == 4) {
        u32 po[1] = {lane < 8 ? lane * 128 : OOR};
        poll_flags<1, A_SLEEP>(rws, po, WS_FA + g8 * REP, get_tag());
        lds_set(SY_W2X, 1);
      } else lds_wait(SY_W2X, 1);
      lds_wait4(SY_KD, 1);
      STAMP(10);
      __builtin_amdgcn_s_setprio(3);
#pragma unroll
      for (int i = 0; i < 2; i++) {
#pragma unroll
        for (int j = 0; j < 2; j++) sa[i][j] = ld16c(rws, lane * 16, WS_STG + j * 4096 + ((wave - 4) * 2 + i) * 1024);
#pragma unroll
        for (int c = 0; c < 3; c++) xa[c][i] = ld16c(rws, lane * 16, WS_XT + min(ch0 + c, 639u) * 4096 + ((wave - 4) * 2 + i) * 1024);
      }
      SB();
      __builtin_amdgcn_s_setprio(S_PRIO);
      const u32 ub = opq(L_SSM + 512 + (wave - 4) * 2048 + lane * 16);
#pragma unroll
      for (int i = 0; i < 2; i++) {
        u32x4 o[3];
#pragma unroll
        for (int j = 0; j < 4; j++) {
          const u32 p0 = sa[i][j >> 1][(j & 1) * 2], p1 = sa[i][j >> 1][(j & 1) * 2 + 1];
          const float r0 = __builtin_amdgcn_rcpf(bfhi(p0)), r1 = __builtin_amdgcn_rcpf(bfhi(p1));
          const float m0 = -bflo(p0) * r0, m1 = __uint_as_float(opq(__float_as_uint(-bflo(p1) * r1)));
#pragma unroll
          for (int c = 0; c < 3; c++) {
            float u0;
            asm("v_fma_f32 %0, %1, %2, %3" : "=v"(u0) : "v"(bflo(xa[c][i][j])), "v"(r0), "v"(m0));
            o[c][j] = pk2(u0, __builtin_fmaf(bfhi(xa[c][i][j]), r1, m1));
          }
        }
#pragma unroll
        for (int c = 0; c < 3; c++) *lp<u32x4>(ub + c * SSM_SZ + i * 1024) = o[c];
      }
      *lp<u32x2>(SY_U + (wave - 4) * 8) = u32x2{1u, 1u};
      CFENCE();
      STAMP(11);
    }
    lds_wait4(SY_U, 1);
    STAMP(7);
    lds_wait(SY_M + (wave - 4) * 4, 13);
    __builtin_amdgcn_s_setprio(2);
    STAMP(4);
    const u32 ub = opq(LU + 32 + 32 * c16 + 16 * gam);
    const u32 g4 = opq(LB + ((tq & 1) ? S3 : S5) - 8 + (108 - 32 * gam - 4 * tq + 4 * tp) * 2);
    const u32 g8a = opq(LB + ((tq & 1) ? S9 : S7) + (216 - 64 * gam - 8 * tq + 4 * tp) * 2);
    const u32 sb = opq(LB + 32 * c16 + 8 * gam);
    constexpr int RING = 8, AHEAD = 6;
    u32x4 uf[RING];
    auto ldf = [&](int n) {
      if (n < 64) uf[n % RING] = *lp<u32x4>(ub + n * 64);
      else if (n < 76) { u32x2 a = trd(g4 + (n - 64) * 256 + 32), b = trd(g4 + (n - 64) * 256); uf[n % RING] = u32x4{a.x, a.y, b.x, b.y}; }
      else if (n < 80) { u32x2 a = trd(g8a + (n - 76) * 512 + 64), b = trd(g8a + (n - 76) * 512); uf[n % RING] = u32x4{a.x, a.y, b.x, b.y}; }
    };
    {
      u32x4 sf[6];
#pragma unroll
      for (int t = 0; t < 6; t++) sf[t] = *lp<u32x4>(ub + 432 + t * 512);
#pragma unroll
      for (int n = 0; n < AHEAD; n++) ldf(n);
      SB();
      f32x4 sr_[2][4];
#pragma unroll
      for (int t = 0; t <= 6; t++) {
#pragma unroll
        for (int wd = 0; wd < 4; wd++) {
          if (t < 6 && wd < (t < 4 ? 4 : 2)) sr_[t & 1][wd] = mf16(fragv(tb[wd]), fragv(sf[t]), f32x4{0, 0, 0, 0});
          if (t > 0 && wd < (t - 1 < 4 ? 4 : 2)) {
            const f32x4 s = sr_[(t - 1) & 1][wd];
            *lp<u32x2>(sb + (wd == 0 ? S3 : wd == 1 ? S5 : wd == 2 ? S7 : S9) + (t - 1) * 512) = u32x2{pk2(s[0], s[1]), pk2(s[2], s[3])};
          }
          SB();
        }
      }
    }
    CFENCE();
    f32x4 y[8];
#pragma unroll
    for (int c = 0; c < 8; c++) y[c] = f32x4{0, 0, 0, 0};
    sfor<80>([&](auto N) {
      constexpr int n = N;
      const bf16x8 f = fragv(uf[n % RING]);
      ldf(n + AHEAD);
      sfor<8>([&](auto C) {
        constexpr int c = C;
        if constexpr (n < 64) {
          constexpr int m = 8 * c - (n - 7);
          if constexpr (m >= 0 && m <= 16) y[c] = mf16(Ff[m], f, y[c]);
          if constexpr (m == 32) y[c] = mf16(Ff[17], f, y[c]);
        } else if constexpr (n < 76) {
          constexpr int e = 2 * c - (n - 64);
          if constexpr (e >= 3 && e <= 8) y[c] = mf16(f, H4[e - 3], y[c]);
        } else {
          constexpr int e = c - (n - 76);
          if constexpr (e >= 4) y[c] = mf16(f, H8[e - 4], y[c]);
        }
      });
      SB();
    });
    CFENCE();
    STAMP(5);
    const u32 yb = opq(LU + 32 * c16 + 8 * gam), yr = opq(LU + lane * 16);
#pragma unroll
    for (int c = 0; c < 8; c++) *lp<u32x2>(yb + c * 512) = u32x2{pk2(y[c][0], y[c][1]), pk2(y[c][2], y[c][3])};
    CFENCE();
    u32x4 ov[4];
#pragma unroll
    for (int i = 0; i < 4; i++) ov[i] = *lp<u32x4>(yr + i * 1024);
    const u32x2 tag = *lp<u32x2>(SY_TAG);
    lds_set(SY_SDONE + (wave - 4) * 4, 1);
    if (wave == 4) lds_set(SY_SDONE + 12, 1);
#pragma unroll
    for (int i = 0; i < 4; i++) { st16c(ov[i], rws, lane * 16 + WS_ST + d * 4096, i * 1024); if (ST_PACE && i < 3) { SB(); __builtin_amdgcn_s_sleep(ST_PACE); SB(); } }
    publish<8>(rws, WS_FC + d * 64, tag, lane);
    STAMP(6);
    if (wave == 4) {
      u32 pc_[2] = {(qn * 64 + lane) * 64, lane < 16 ? (512 + qn * 16 + lane) * 64 : OOR};
      lds_wait4(SY_SDONE, 1);
      __builtin_amdgcn_s_sleep(C_DELAY);
      poll_flags<2, C_SLEEP>(rws, pc_, WS_FC + g8 * REP, tag);
      STAMP(8);
      u32x4 sv[10];
#pragma unroll
      for (int i = 0; i < 10; i++) {
        const u32 d0 = i < 8 ? qn * 64 + i * 8 : 512 + qn * 16 + (i - 8) * 8;
        sv[i] = ld16c(rws, (lane >> 3) * 4096 + (lane & 7) * 16, WS_ST + d0 * 4096 + qm * 128);
      }
      lds_wait4(SY_M, 17);
      const u32 sb2 = opq(L_SIMG + (lane >> 3) * 128 + (((lane & 7) ^ (((lane >> 4) & 3) << 1)) << 4));
#pragma unroll
      for (int i = 0; i < 10; i++) *lp<u32x4>(sb2 + i * 1024) = sv[i];
      lds_wait4(SY_M, 18);
      lds_set(SY_EPI2, 1);
      STAMP(9);
    }
  } else {
    const rsrc_t rw2 = mk(w2, 1536 * 1280);
    const u32 io = (w >> 4) * 4 + (g8 >> 1);
    const u32 kr = min(io / 12, 4u), lg = (g8 & 1) * 12 + io % 12;
    __builtin_amdgcn_s_setprio(2);
    u32x4 e[3], v[16];
    lds_set(SY_SDONE + 8, 1);
    lds_set(SY_KD + 8, 1);
    __builtin_amdgcn_s_sleep(P_DELAY);
#pragma unroll
    for (int i = 0; i < 3; i++) {
      const u32 c = i * 64 + lane, q = c / 24, rr = c - q * 24;
      e[i] = ld16(rw2, (rr >> 1) * 1280 + q * 32 + (rr & 1) * 16, pair * 12 * 1280 + 1024);
    }
#pragma unroll
    for (int i = 0; i < 16; i++) v[i] = ld16(rw1, (lane >> 3) * 3072 + (lane & 7) * 16, (kr * 128 + i * 8) * 3072 + lg * 128);
    SB();
#pragma unroll
    for (int i = 0; i < 3; i++) {
      const u32 c = i * 64 + lane, q = c / 24, rr = c - q * 24;
      st16c(e[i], rws, q * 49152 + rr * 16, WS_W2X + pair * 384);
    }
    SB();
    float s[8];
#pragma unroll
    for (int j = 0; j < 8; j++) s[j] = 0.f;
#pragma unroll
    for (int i = 0; i < 16; i++)
#pragma unroll
      for (int j = 0; j < 4; j++) { s[2 * j] += bflo(v[i][j]); s[2 * j + 1] += bfhi(v[i][j]); }
#pragma unroll
    for (int j = 0; j < 8; j++) s[j] = swap32_add(swap16_add(dpp_add<0x128>(s[j])));
    {
      const f32x4 o = (lane & 8) ? f32x4{s[4], s[5], s[6], s[7]} : f32x4{s[0], s[1], s[2], s[3]};
      st16c(__builtin_bit_cast(u32x4, o), rws, (lane < 16 && io < 60) ? (lane & 7) * 32 + (lane >> 3) * 16 + WS_CSP + kr * 6144 + lg * 256 : OOR, 0);
    }
    const u32x2 tag = get_tag();
    STAMP(1);
    publish<1>(rws, WS_F1 + g8 * REP + (32 + (w >> 4)) * 64, tag, lane);
    if (r5 == 1) {
      u32 po[1] = {lane < 48 ? lane * 64 : OOR};
      poll_flags<1, 2>(rws, po, WS_F1 + g8 * REP, tag);
      publish<8>(rws, WS_FA + g8 * 128, tag, lane);
      STAMP(3);
    }
    STAMP(2);
  }
  STAMPS_OUT();
}
extern "C" __device__ __attribute__((used, noinline)) void s4mlp_end() {}

extern "C" void kernel_launch(void* const* d_in, const int* in_sizes, int n_in, void* d_out, int out_size, void* d_ws, size_t ws_size,
                              hipStream_t stream) {
  s4mlp<<<256, 512, 0, stream>>>((const u16*)d_in[0], (const u16*)d_in[1], (const u16*)d_in[2], (const u16*)d_in[3], (const u16*)d_in[4],
                                 (const u16*)d_in[5], (const u16*)d_in[6], (const u16*)d_in[7], (const u16*)d_in[8], (const u16*)d_in[10],
                                 (const u16*)d_in[11], (const u16*)d_in[12], (u16*)d_out, (char*)d_ws);
}
```

```cpp
#define PROBE 1
#define PROLE (wave == 4)
#define W2_PF 4
#define NO_W1_PF
#define M_POLL
#include <hip/hip_runtime.h>
#include <stdint.h>
#include <utility>

typedef unsigned short u16;
typedef unsigned u32;
typedef unsigned long long u64;
typedef __attribute__((ext_vector_type(2))) float f32x2;
typedef __attribute__((ext_vector_type(4))) float f32x4;
typedef __attribute__((ext_vector_type(16))) float f32x16;
typedef __attribute__((ext_vector_type(4))) short s16x4;
typedef __attribute__((ext_vector_type(2))) __bf16 bf16x2;
typedef __attribute__((ext_vector_type(8))) __bf16 bf16x8;
typedef __attribute__((ext_vector_type(2))) _Float16 f16x2;
typedef __attribute__((ext_vector_type(8))) _Float16 f16x8;
typedef __attribute__((ext_vector_type(2))) u32 u32x2;
typedef __attribute__((ext_vector_type(4))) u32 u32x4;
typedef __amdgpu_buffer_rsrc_t rsrc_t;
#define DI __device__ __forceinline__
#define LAS __attribute__((address_space(3)))
#define SPIN_NAP()
#define CFENCE() asm volatile("" ::: "memory")
#define SB() __builtin_amdgcn_sched_barrier(0)

constexpr u32 REP = 65536 + 256;
constexpr u32 WS_EPOCH = 0, WS_REC = 1u << 19, WS_STG = 32768, WS_CSP = 65536, WS_FB = (14u << 20) + 524288, WS_FA = 15u << 20, WS_F1 = WS_FA + 4096, WS_FC = WS_FA + 24576,
              WS_W2X = 131072, WS_XT = 1u << 20, WS_ST = 4u << 20, WS_ACT = 8u << 20, WS_DBG = 16u << 20, WS_USED = 24u << 20;
constexpr u32 L_SSM = 0, SSM_SZ = 15488, L_XS = L_SSM + 3 * SSM_SZ, L_CS = L_XS + 10240, L_B1 = L_CS + 384, L_ST = L_B1 + 384,
              L_SY = L_ST + 1024, L_DUMMY = L_SY + 512, L_END = L_DUMMY + 512, RB = 61440;
constexpr u32 SY_M = L_SY, SY_TAG = L_SY + 16, SY_TAGOK = L_SY + 24, SY_EPI1 = L_SY + 80, SY_U = L_SY + 96, SY_FULL = L_SY + 36,
              SY_EPI2 = L_SY + 40, SY_W2X = L_SY + 44, SY_SDONE = L_SY + 48, SY_RED = L_SY + 64, SY_XF = L_SY + 112, SY_RS = L_SY + 224,
              SY_F0 = L_SY + 256, SY_KD = L_SY + 272, SY_B = L_SY + 288, SY_F = L_SY + 128;
constexpr u32 L_SIMG = RB, L_RED = RB + 16384 + 4096;
static_assert((L_RED & 16383) + 10240 <= 16384 && L_RED + 16384 + 10240 <= RB + 49152, "red");
static_assert(L_END <= RB && RB + 102400 <= 163840, "lds");
constexpr u32 SPIN_CAP = 1u << 17, LSPIN_CAP = 1u << 22;
#define W2_PF_AT 7
#define PK_ROUNDS 1
#define POLL_AT 7
constexpr float LOG2E = 1.4426950408889634f, LN2 = 0.6931471805599453f;

constexpr u32 T4 = 5376, T8 = 5888, S5 = 16, S3 = 3216, S9 = 6400, S7 = 8752, LB_DUM = 7168;
__shared__ __attribute__((aligned(1024))) char smem[163840];

template <class F, int... I> DI void sfor_(F&& f, std::integer_sequence<int, I...>) { (f(std::integral_constant<int, I>{}), ...); }
template <int N, class F> DI void sfor(F&& f) { sfor_(f, std::make_integer_sequence<int, N>{}); }
template <class T> DI LAS T* lp(u32 off) { return (LAS T*)((LAS char*)smem + off); }
DI u32 opq(u32 x) { asm volatile("" : "+v"(x)); return x; }
DI u32 rfl(u32 x) { return __builtin_amdgcn_readfirstlane(x); }
DI float bflo(u32 v) { return __uint_as_float(v << 16); }
DI float bfhi(u32 v) { return __uint_as_float(v & 0xffff0000u); }
DI u32 pk2(float a, float b) { bf16x2 v = {(__bf16)a, (__bf16)b}; return __builtin_bit_cast(u32, v); }
DI float rbf(float a) { return (float)(__bf16)a; }
DI rsrc_t mk(const void* p, u32 n) { return __builtin_amdgcn_make_buffer_rsrc((void*)p, (short)0, (int)n, 0x00020000); }
DI u32x4 ld16c(rsrc_t r, u32 vo, u32 so) { return __builtin_amdgcn_raw_buffer_load_b128(r, vo, so, 16); }
DI u32x2 ld8c(rsrc_t r, u32 vo, u32 so) { return __builtin_amdgcn_raw_buffer_load_b64(r, vo, so, 16); }
DI u32 ld4c(rsrc_t r, u32 vo, u32 so) { return __builtin_amdgcn_raw_buffer_load_b32(r, vo, so, 16); }
DI void st16c(u32x4 v, rsrc_t r, u32 vo, u32 so) { __builtin_amdgcn_raw_buffer_store_b128(v, r, vo + so, 0, 16); }
DI void st16(u32x4 v, rsrc_t r, u32 vo, u32 so) { __builtin_amdgcn_raw_buffer_store_b128(v, r, vo + so, 0, 0); }
DI void st8c(u32x2 v, rsrc_t r, u32 vo, u32 so) { __builtin_amdgcn_raw_buffer_store_b64(v, r, vo, so, 16); }
DI void st4c(u32 v, rsrc_t r, u32 vo, u32 so) { __builtin_amdgcn_raw_buffer_store_b32(v, r, vo, so, 16); }
DI u32x4 ld16(rsrc_t r, u32 vo, u32 so) { return __builtin_amdgcn_raw_buffer_load_b128(r, vo, so, 0); }
#define VMCNT(n) asm volatile("s_waitcnt vmcnt(" #n ")" ::: "memory")

DI void dma(u32 lds, u32 vo, rsrc_t r, u32 so) {
  asm volatile("s_mov_b32 m0, %0\n\ts_nop 0\n\tbuffer_load_dwordx4 %1, %2, %3 offen lds" ::"s"(lds), "v"(vo), "s"(r), "s"(so));
}
template <int OFF> DI void dma_next(u32 vo, rsrc_t r, u32 so) {
  asm volatile("buffer_load_dwordx4 %0, %1, %2 offen offset:%3 lds" ::"v"(vo), "s"(r), "s"(so), "i"(OFF));
}
DI void dma_flag(u32 lds, rsrc_t r) {
  asm volatile("s_mov_b64 exec, 1\n\ts_mov_b32 m0, %0\n\ts_nop 0\n\tbuffer_load_dword off, %1, 0 lds\n\ts_mov_b64 exec, -1" ::"s"(lds), "s"(r) : "memory");
}
DI void dma3(u32 lds, u32 v0, u32 v1, u32 v2, rsrc_t r, u32 so) {
  asm volatile("s_mov_b32 m0, %0\n\ts_nop 0\n\tbuffer_load_dwordx4 %1, %4, %5 offen lds\n\tbuffer_load_dwordx4 %2, %4, %5 offen offset:1024 lds\n\t"
               "buffer_load_dwordx4 %3, %4, %5 offen offset:2048 lds" ::"s"(lds), "v"(v0), "v"(v1), "v"(v2), "s"(r), "s"(so));
}
DI void dma4(u32 lds, u32 v0, u32 v1, u32 v2, u32 v3, rsrc_t r, u32 so) {
  asm volatile("s_mov_b32 m0, %0\n\ts_nop 0\n\tbuffer_load_dwordx4 %1, %5, %6 offen lds\n\tbuffer_load_dwordx4 %2, %5, %6 offen offset:1024 lds\n\t"
               "buffer_load_dwordx4 %3, %5, %6 offen offset:2048 lds\n\tbuffer_load_dwordx4 %4, %5, %6 offen offset:3072 lds"
               ::"s"(lds), "v"(v0), "v"(v1), "v"(v2), "v"(v3), "s"(r), "s"(so));
}
DI void dmac(u32 lds, u32 vo, rsrc_t r, u32 so) {
  asm volatile("s_mov_b32 m0, %0\n\ts_nop 0\n\tbuffer_load_dwordx4 %1, %2, %3 offen sc1 lds" ::"s"(lds), "v"(vo), "s"(r), "s"(so));
}
DI void dma2c(u32 lds, u32 vo, rsrc_t r, u32 so) {
  asm volatile("s_mov_b32 m0, %0\n\ts_nop 0\n\tbuffer_load_dwordx4 %1, %2, %3 offen sc1 lds\n\tbuffer_load_dwordx4 %1, %2, %3 offen offset:1024 sc1 lds"
               ::"s"(lds), "v"(vo), "s"(r), "s"(so));
}
DI u32x2 trd(u32 off) { return __builtin_bit_cast(u32x2, __builtin_amdgcn_ds_read_tr16_b64_v4i16(lp<s16x4>(off))); }
DI bf16x8 frag(u32x2 a, u32x2 b) { u32x4 v = {a.x, a.y, b.x, b.y}; return __builtin_bit_cast(bf16x8, v); }
DI bf16x8 fragv(u32x4 v) { return __builtin_bit_cast(bf16x8, v); }
DI f32x4 mf16(bf16x8 a, bf16x8 b, f32x4 c) { return __builtin_amdgcn_mfma_f32_16x16x32_bf16(a, b, c, 0, 0, 0); }
DI f32x16 mf32(bf16x8 a, bf16x8 b, f32x16 c) { return __builtin_amdgcn_mfma_f32_32x32x16_bf16(a, b, c, 0, 0, 0); }

DI void lds_set(u32 off, u32 v) { CFENCE(); *(volatile LAS u32*)lp<u32>(off) = v; CFENCE(); }
DI void lds_wait(u32 off, u32 v) {
  CFENCE();
#pragma nounroll
  for (u32 n = 0; rfl(*(volatile LAS u32*)lp<u32>(off)) < v && n < LSPIN_CAP; n++) __builtin_amdgcn_s_sleep(1);
  CFENCE();
}
DI u32 lds_min4(u32 off) {
  CFENCE();
  u32x4 x = *(volatile LAS u32x4*)lp<u32x4>(off);
  u32 m = rfl(min(min(x.x, x.y), min(x.z, x.w)));
  CFENCE();
  return m;
}
DI void lds_wait4(u32 off, u32 v) {
#pragma nounroll
  for (u32 n = 0; lds_min4(off) < v && n < LSPIN_CAP; n++) __builtin_amdgcn_s_sleep(1);
}
DI void lds_spin4(u32 off, u32 v) {
#pragma nounroll
  for (u32 n = 0; lds_min4(off) < v && n < LSPIN_CAP; n++) { SPIN_NAP(); }
}
DI u32x2 get_tag() { lds_wait(SY_TAGOK, 1); return *lp<u32x2>(SY_TAG); }
#define A_SLEEP 4
#define A_DELAY 16
#define C_DELAY 30
#define PARK_AT 12
#define G2_WIN 7
#define XT_PACE 2
#define ST_PACE 0
#ifndef M_PRIO
#define M_PRIO 0
#endif
#define S_PRIO 1
#define L_DELAY 4
#define P_DELAY 1
#define BULK_DELAY 5
#define VMX "29"
#define YIELD_AT 8
#define G_DELAY 1
#define G_SLEEP 2
#define B_DELAY 30
#define B_SLEEP 10
#define C_SLEEP 12
constexpr u32 OOR = 0x7ffffff0u;
template <int NL, int SLEEP> DI void poll_flags(rsrc_t ws, u32 (&off)[NL], u32 so, u32x2 tag) {
  u64 pend[NL];
#pragma unroll
  for (int i = 0; i < NL; i++) pend[i] = 1;
#pragma nounroll
  for (u32 spin = 0; spin < SPIN_CAP; spin++) {
    CFENCE();
    u32x2 v[NL];
#pragma unroll
    for (int i = 0; i < NL; i++) { v[i] = tag; if (NL == 1 || pend[i]) v[i] = ld8c(ws, off[i], so); }
    u64 any = 0;
#pragma unroll
    for (int i = 0; i < NL; i++) {
      if (v[i].x == tag.x && v[i].y == tag.y) off[i] = OOR;
      pend[i] = __builtin_amdgcn_ballot_w64(off[i] != OOR);
      any |= pend[i];
    }
    if (any == 0) break;
    __builtin_amdgcn_s_sleep(SLEEP);
  }
  CFENCE();
}
template <int NL, int SLEEP> DI void poll_packed(rsrc_t ws, u32 vo, u32 so, u32x2 tag) {
  u32 off[NL];
#pragma unroll
  for (int i = 0; i < NL; i++) off[i] = vo + i * 1024;
#pragma nounroll
  for (u32 spin = 0; spin < SPIN_CAP; spin++) {
    CFENCE();
    u32x4 v[NL];
#pragma unroll
    for (int i = 0; i < NL; i++) v[i] = ld16c(ws, off[i], so);
    bool pend = false;
#pragma unroll
    for (int i = 0; i < NL; i++) {
      if (v[i].x == tag.x && v[i].y == tag.y && v[i].z == tag.x && v[i].w == tag.y) off[i] = OOR;
      pend = pend || off[i] != OOR;
    }
    if (__builtin_amdgcn_ballot_w64(pend) == 0) break;
    __builtin_amdgcn_s_sleep(SLEEP);
  }
  CFENCE();
}
template <int R> DI void publish(rsrc_t ws, u32 so, u32x2 tag, int lane) {
  VMCNT(0);
  st8c(tag, ws, lane < R ? lane * REP : OOR, so);
  CFENCE();
}

#define STAMP(i) do { if ((i) == PROBE) prb_ = (u32)(__builtin_amdgcn_s_memrealtime() - rt0_); } while (0)
#define STAMPS_DECL const u64 rt0_ = __builtin_amdgcn_s_memrealtime(); u32 prb_ = 0
#define STAMPS_GO
#define STAMPS_OUT() do { const u32 ex_ = (PROLE) ? prb_ : 0; VMCNT(0); while (__builtin_amdgcn_s_memrealtime() - rt0_ < 3500 + ex_) __builtin_amdgcn_s_sleep(2); } while (0)

template <bool PK> DI f32x2 cmul(f32x2 a, f32x2 b) {
  if constexpr (PK) {
    f32x2 bs = {-b.y, b.x};
    f32x2 r = a.xx * b;
    return __builtin_elementwise_fma(a.yy, bs, r);
  } else {
    float t; asm("v_mul_f32 %0, %1, %2" : "=v"(t) : "v"(a.x), "v"(b.x));
    return f32x2{__builtin_fmaf(-a.y, b.y, t), __builtin_fmaf(a.y, b.x, a.x * b.y)};
  }
}
DI f32x2 csq(f32x2 a) {
  float t; asm("v_mul_f32 %0, %1, %1" : "=v"(t) : "v"(a.x));
  return f32x2{__builtin_fmaf(-a.y, a.y, t), (a.x + a.x) * a.y};
}
DI u32 toh(f32x2 v) { u32 r; asm("v_cvt_pk_f16_f32 %0, %1, %2" : "=v"(r) : "v"(v.x), "v"(v.y)); return r; }
template <bool CONJ> DI void hmul2(u32& r0, u32 a0, u32 b0, u32& r1, u32 a1, u32 b1) {
  if constexpr (!CONJ)
    asm("v_pk_mul_f16 %0, %2, %3 op_sel_hi:[0,1]\n\tv_pk_mul_f16 %1, %4, %5 op_sel_hi:[0,1]\n\t"
        "v_pk_fma_f16 %0, %2, %3, %0 op_sel:[1,1,0] op_sel_hi:[1,0,1] neg_lo:[0,1,0]\n\tv_pk_fma_f16 %1, %4, %5, %1 op_sel:[1,1,0] op_sel_hi:[1,0,1] neg_lo:[0,1,0]"
        : "=&v"(r0), "=&v"(r1) : "v"(a0), "v"(b0), "v"(a1), "v"(b1));
  else
    asm("v_pk_mul_f16 %0, %2, %3 op_sel_hi:[0,1] neg_hi:[0,1]\n\tv_pk_mul_f16 %1, %4, %5 op_sel_hi:[0,1] neg_hi:[0,1]\n\t"
        "v_pk_fma_f16 %0, %2, %3, %0 op_sel:[1,1,0] op_sel_hi:[1,0,1]\n\tv_pk_fma_f16 %1, %4, %5, %1 op_sel:[1,1,0] op_sel_hi:[1,0,1]"
        : "=&v"(r0), "=&v"(r1) : "v"(a0), "v"(b0), "v"(a1), "v"(b1));
}
DI f32x4 mfh(u32x4 a, u32x4 b, f32x4 c) { return __builtin_amdgcn_mfma_f32_16x16x32_f16(__builtin_bit_cast(f16x8, a), __builtin_bit_cast(f16x8, b), c, 0, 0, 0); }
DI f32x2 cexp2(float lr, float th, float sgn) {
  float m = __builtin_amdgcn_exp2f(lr), f = __builtin_amdgcn_fractf(th);
  f32x2 r = {m * __builtin_amdgcn_cosf(f), sgn * m * __builtin_amdgcn_sinf(f)};
  return r;
}
template <int CTRL> DI float dpp_add(float v) {
  return v + __builtin_bit_cast(float, __builtin_amdgcn_update_dpp(0, __builtin_bit_cast(int, v), CTRL, 0xf, 0xf, true));
}
template <int CTRL> DI float dpp_max(float v) {
  return fmaxf(v, __builtin_bit_cast(float, __builtin_amdgcn_update_dpp(0, __builtin_bit_cast(int, v), CTRL, 0xf, 0xf, true)));
}
DI float wave_max(float v) {
  v = dpp_max<0x128>(v); v = dpp_max<0x124>(v); v = dpp_max<0x122>(v); v = dpp_max<0x121>(v);
  const float a = __builtin_bit_cast(float, __builtin_amdgcn_readlane(__builtin_bit_cast(int, v), 0)), b = __builtin_bit_cast(float, __builtin_amdgcn_readlane(__builtin_bit_cast(int, v), 16)),
              c = __builtin_bit_cast(float, __builtin_amdgcn_readlane(__builtin_bit_cast(int, v), 32)), d = __builtin_bit_cast(float, __builtin_amdgcn_readlane(__builtin_bit_cast(int, v), 48));
  return fmaxf(fmaxf(a, b), fmaxf(c, d));
}
DI float swap16_add(float v) { auto r = __builtin_amdgcn_permlane16_swap(__float_as_uint(v), __float_as_uint(v), false, false); return __uint_as_float(r[0]) + __uint_as_float(r[1]); }
DI float swap32_add(float v) { auto r = __builtin_amdgcn_permlane32_swap(__float_as_uint(v), __float_as_uint(v), false, false); return __uint_as_float(r[0]) + __uint_as_float(r[1]); }
DI float red32(float v) {
  v = dpp_add<0x128>(v); v = dpp_add<0x124>(v); v = dpp_add<0x122>(v); v = dpp_add<0x121>(v);
  return swap16_add(v);
}
DI u32x4 ldflags4(u32 off) { CFENCE(); u32x4 x = *(volatile LAS u32x4*)lp<u32x4>(off); CFENCE(); return x; }
DI u32 ldflag(u32 off) { CFENCE(); u32 x = *(volatile LAS u32*)lp<u32>(off); CFENCE(); return x; }
DI void chk4(u32x4 x, u32 off, u32 v) {
  if (rfl(min(min(x.x, x.y), min(x.z, x.w))) < v) {
#pragma nounroll
    for (u32 n = 0; lds_min4(off) < v && n < LSPIN_CAP; n++) { SPIN_NAP(); }
  }
  CFENCE();
}
DI void chk1(u32 x, u32 off, u32 v) {
  if (rfl(x) < v) {
#pragma nounroll
    for (u32 n = 0; rfl(ldflag(off)) < v && n < LSPIN_CAP; n++) { SPIN_NAP(); }
  }
  CFENCE();
}

template <int NR> DI void kgen(const u32 (&pw)[6], u32 ldt, u32 LB, u32 lane, f32x4 (&ka)[2][2]) {
  const u32 c16 = lane & 15, gam = lane >> 4;
  const u32 sw = opq(LB + lane * 4), sr = opq(LB + c16 * 288 + gam * 16), sr2 = opq(LB + (16 + min(c16, 4u)) * 288 + gam * 16);
  const u32 sqa = opq(LB + (21 + (c16 & 3)) * 288 + gam * 16), sqb = opq(LB + (c16 < 4 ? 21 : 24 + (c16 >> 2)) * 288 + gam * 16), sqc = opq(LB + 28 * 288 + gam * 16);
  *lp<u32>(sw + 21 * 288) = 0x3c00u;
  SB();
  const float dt = rbf(__builtin_amdgcn_exp2f(bflo(ldt) * LOG2E));
#pragma unroll
  for (int i = 0; i < 4; i++) ka[i >> 1][i & 1] = f32x4{0, 0, 0, 0};
  u32x4 qa[4], qb_[4], qc[4], pf[2][4];
  auto kq = [&]() {
    u32x4 q0[4], q1[4];
#pragma unroll
    for (int k = 0; k <= 4; k++) {
      u32 a0, a1, a2, a3, b0, b1_, b2, b3;
      if (k < 4) hmul2<false>(a0, qa[k].x, qb_[k].x, a1, qa[k].y, qb_[k].y);
      else asm volatile("s_nop 1" : "+v"(q1[3]));
      SB();
      if (k > 0) ka[0][0] = mfh(pf[0][k - 1], q0[k - 1], ka[0][0]);
      SB();
      if (k < 4) hmul2<false>(a2, qa[k].z, qb_[k].z, a3, qa[k].w, qb_[k].w);
      SB();
      if (k > 0) ka[1][0] = mfh(pf[1][k - 1], q0[k - 1], ka[1][0]);
      SB();
      if (k < 4) hmul2<false>(b0, a0, qc[k].x, b1_, a1, qc[k].y);
      SB();
      if (k > 0) ka[0][1] = mfh(pf[0][k - 1], q1[k - 1], ka[0][1]);
      SB();
      if (k < 4) hmul2<false>(b2, a2, qc[k].z, b3, a3, qc[k].w);
      SB();
      if (k > 0) ka[1][1] = mfh(pf[1][k - 1], q1[k - 1], ka[1][1]);
      SB();
      if (k < 4) { q0[k] = u32x4{a0, a1, a2, a3}; q1[k] = u32x4{b0, b1_, b2, b3}; }
    }
  };
  auto prm = [&](int r, int i) { return r == 0 ? bflo(pw[i]) : bfhi(pw[i]); };
  u32 wh[NR];
  float unscale;
  {
    f32x2 wv[NR];
    float mx = 0.f;
#pragma unroll
    for (int r = 0; r < NR; r++) {
      wv[r] = cmul<false>(f32x2{prm(r, 2), -prm(r, 3)}, f32x2{prm(r, 4), -prm(r, 5)});
      mx = fmaxf(mx, fmaxf(fabsf(wv[r].x), fabsf(wv[r].y)));
    }
    const int ex = __builtin_amdgcn_frexp_expf(wave_max(mx));
#pragma unroll
    for (int r = 0; r < NR; r++) wh[r] = toh(f32x2{__builtin_amdgcn_ldexpf(wv[r].x, 14 - ex), __builtin_amdgcn_ldexpf(wv[r].y, 14 - ex)});
    unscale = __builtin_amdgcn_ldexpf(dt * LN2, ex - 14);
  }
  sfor<NR>([&](auto R) {
    constexpr int r = R;
    const float lr = dt * prm(r, 0) * LOG2E, th = dt * prm(r, 1) * 0.15915494309189535f;
    u32 zh[11];
    {
      f32x2 za = cexp2(lr, th, 1.f), zb = cexp2(lr * 32.f, th * 32.f, 1.f);
      zh[0] = toh(za); zh[5] = toh(zb);
#pragma unroll
      for (int i = 1; i < 6; i++) { if (i < 5) { za = csq(za); zh[i] = toh(za); } zb = csq(zb); zh[5 + i] = toh(zb); }
    }
    u32 p[21], z24, z96;
    p[0] = wh[r];
    hmul2<false>(z24, zh[3], zh[4], z96, zh[5], zh[6]);
    *lp<u32>(sw + 22 * 288) = zh[3]; *lp<u32>(sw + 23 * 288) = zh[4]; *lp<u32>(sw + 24 * 288) = z24; *lp<u32>(sw + 25 * 288) = zh[5];
    *lp<u32>(sw + 26 * 288) = zh[6]; *lp<u32>(sw + 27 * 288) = z96; *lp<u32>(sw + 28 * 288) = zh[7];
    hmul2<true>(p[1], p[0], zh[0], p[2], p[0], zh[1]);
    hmul2<true>(p[4], p[0], zh[2], p[8], p[0], zh[8]);
    hmul2<true>(p[12], p[0], zh[9], p[16], p[0], zh[10]);
    hmul2<true>(p[3], p[1], zh[1], p[5], p[1], zh[2]);
    hmul2<true>(p[6], p[2], zh[2], p[9], p[2], zh[8]);
    hmul2<true>(p[10], p[4], zh[8], p[13], p[4], zh[9]);
    hmul2<true>(p[14], p[8], zh[9], p[17], p[8], zh[10]);
    hmul2<true>(p[18], p[12], zh[10], p[20], p[16], zh[10]);
    hmul2<true>(p[7], p[3], zh[2], p[11], p[6], zh[8]);
    hmul2<true>(p[15], p[10], zh[9], p[19], p[14], zh[10]);
    SB();
#pragma unroll
    for (int i = 0; i < 21; i++) *lp<u32>(sw + i * 288) = p[i];
    CFENCE();
    if (r > 0) { SB(); kq(); }
    SB();
#pragma unroll
    for (int k = 0; k < 4; k++) { qa[k] = *lp<u32x4>(sqa + k * 64); qb_[k] = *lp<u32x4>(sqb + k * 64); qc[k] = *lp<u32x4>(sqc + k * 64); pf[0][k] = *lp<u32x4>(sr + k * 64); pf[1][k] = *lp<u32x4>(sr2 + k * 64); }
    CFENCE();
    SB();
  });
  kq();
#pragma unroll
  for (int i = 0; i < 4; i++) ka[i >> 1][i & 1] *= unscale;
}

extern "C" __global__ void __launch_bounds__(512, 2)
s4mlp(const u16* __restrict__ x, const u16* __restrict__ Are, const u16* __restrict__ Aim, const u16* __restrict__ Bre,
      const u16* __restrict__ Bim, const u16* __restrict__ Cre, const u16* __restrict__ Cim, const u16* __restrict__ logdt,
      const u16* __restrict__ Dsk, const u16* __restrict__ w1, const u16* __restrict__ b1, const u16* __restrict__ w2,
      u16* __restrict__ out, char* __restrict__ wsp) {
  const int lane = threadIdx.x & 63;
  const u32 wave = rfl(threadIdx.x >> 6);
  const u32 w = blockIdx.x, g8 = w & 7, r5 = w >> 3;
  STAMPS_DECL;
  rsrc_t rc;
  {
    u64 pc; u32 back, len;
    asm volatile("s_getpc_b64 %0\n1:\n\ts_mov_b32 %1, 1b-s4mlp\n\ts_mov_b32 %2, s4mlp_end-s4mlp" : "=s"(pc), "=s"(back), "=s"(len));
    rc = mk((const char*)(pc - back), len);
  }
  asm volatile("s_cmp_lg_u32 %3, 0\n\ts_cbranch_scc1 2f\n\ts_mov_b32 m0, %0\n\ts_nop 0\n\tbuffer_load_dword %1, %2, 0 offen lds\n2:"
               ::"s"(L_DUMMY), "v"(((threadIdx.x << 3) + (r5 & 7)) << 7), "s"(rc), "s"(wave) : "scc");
  *lp<u32x2>(L_SY + lane * 8) = u32x2{0, 0};
  const u32 pm = (g8 >> 1) * 4 + (r5 >> 3), pn = (g8 & 1) * 8 + (r5 & 7);
  const u32 qm = g8 * 4 + (r5 >> 3), qn = r5 & 7;
  const u32 pair = (w >> 4) * 8 + g8, odd = r5 & 1;
  const u32 ch0 = pair * 5 + odd * 3, nch = 3 - odd;
  const u32 rot = ((r5 >> 3) ^ r5) & 1;
  constexpr u32 rot1 = 0;
  auto ck = [&](u32 j) { return j < 6 ? j + 6 * rot : j - 6 * rot; };
  const rsrc_t rws = mk(wsp, WS_USED);
  const rsrc_t rx = mk(x, 2048 * 1280);
  const u32 c16 = lane & 15, gam = lane >> 4, tq = (lane >> 2) & 3, tp = lane & 3;

  const u32 tm = pm * 2 + (pn >> 3), tn = pn & 7;
  u32 vx[4];
#pragma unroll
  for (int j = 0; j < 4; j++) vx[j] = (lane >> 3) * 1280 + (((lane & 7) ^ (4 * (j & 1) + (lane >> 4))) << 4) + j * 9216;
  const u32 ldx = RB + wave * 4096;
  const u32 soxr[2] = {(pm * 128 + wave * 32) * 1280 + rot1 * 640, (pm * 128 + wave * 32) * 1280 - rot1 * 640};
  auto pieceX = [&](int i, int s) {
    if (i == 0) dma(ldx + ((s + 2) % 3) * 16384, vx[0], rx, soxr[s >= 5] + s * 128);
    if (i == 1) dma_next<1024>(vx[1], rx, soxr[s >= 5] + s * 128);
    if (i == 2) dma_next<2048>(vx[2], rx, soxr[s >= 5] + s * 128);
    if (i == 3) dma_next<3072>(vx[3], rx, soxr[s >= 5] + s * 128);
  };
  const rsrc_t rw1 = mk(w1, 640 * 3072);
  u32 vw[3];
#pragma unroll
  for (int j = 0; j < 3; j++) vw[j] = (((64 * j + lane) * 171) >> 11) * 2880 + lane * 16;
  const u32 d = min(ch0 + (wave & 3), 639u);
  u32 pw[6], ldt2 = 0, dsk2 = 0;
  asm volatile("" : "+v"(vx[0]), "+v"(vx[1]), "+v"(vx[2]), "+v"(vx[3]), "+v"(vw[0]), "+v"(vw[1]), "+v"(vw[2]));
  asm volatile("s_waitcnt lgkmcnt(0)\n\ts_barrier" ::"s"(x), "s"(Are), "s"(Aim), "s"(Bre), "s"(Bim), "s"(Cre), "s"(Cim), "s"(logdt), "s"(Dsk), "s"(w1), "s"(b1), "s"(w2), "s"(out), "s"(wsp) : "memory");
  STAMPS_GO;
  STAMP(0);
  if ((wave & 3) < nch) {
    const u32 ob = d * 384, o = wave < 4 ? 256 + lane * 2 : lane * 4, od = (d >> 1) * 4;
    const char *p0 = (const char*)Are + ob, *p1 = (const char*)Aim + ob, *p2 = (const char*)Bre + ob, *p3 = (const char*)Bim + ob, *p4 = (const char*)Cre + ob, *p5 = (const char*)Cim + ob;
    asm volatile("global_load_dword %0, %2, %3\n\tglobal_load_dword %1, %2, %4" : "=&v"(ldt2), "=&v"(dsk2) : "v"(od), "s"(logdt), "s"(Dsk));
    if (wave < 4)
      asm volatile("global_load_ushort %2, %6, %9\n\tglobal_load_ushort %3, %6, %10\n\tglobal_load_ushort %4, %6, %11\n\tglobal_load_ushort %5, %6, %12\n\tglobal_load_ushort %0, %6, %7\n\tglobal_load_ushort %1, %6, %8"
                   : "=&v"(pw[0]), "=&v"(pw[1]), "=&v"(pw[2]), "=&v"(pw[3]), "=&v"(pw[4]), "=&v"(pw[5]) : "v"(o), "s"(p0), "s"(p1), "s"(p2), "s"(p3), "s"(p4), "s"(p5));
    else
      asm volatile("global_load_dword %2, %6, %9\n\tglobal_load_dword %3, %6, %10\n\tglobal_load_dword %4, %6, %11\n\tglobal_load_dword %5, %6, %12\n\tglobal_load_dword %0, %6, %7\n\tglobal_load_dword %1, %6, %8"
                   : "=&v"(pw[0]), "=&v"(pw[1]), "=&v"(pw[2]), "=&v"(pw[3]), "=&v"(pw[4]), "=&v"(pw[5]) : "v"(o), "s"(p0), "s"(p1), "s"(p2), "s"(p3), "s"(p4), "s"(p5));
  }
  __builtin_amdgcn_s_sleep(BULK_DELAY);
  if (wave < 4) {
    dma3(RB + 49152 + 2 * 12288 + wave * 3072, vw[0], vw[1], vw[2], rw1, pn * 192 + 5 * rot1 * (64 * 3072) + wave * 16 * 3072);
#pragma unroll
    for (int i = 0; i < 4; i++) pieceX(i, 0);
    dma_flag(SY_F0 + wave * 4, rc);
  }
  asm volatile("s_cmp_lt_u32 %8, 4\n\ts_cbranch_scc1 3f\n\ts_waitcnt vmcnt(0)\n3:\n\ts_waitcnt vmcnt(8)"
               : "+v"(pw[0]), "+v"(pw[1]), "+v"(pw[2]), "+v"(pw[3]), "+v"(pw[4]), "+v"(pw[5]), "+v"(ldt2), "+v"(dsk2) : "s"(wave) : "memory", "scc");
  const u32 ldt = ldt2 >> ((d & 1) * 16), dsk = dsk2 >> ((d & 1) * 16);
  asm volatile("" ::: "memory");
  const u32 wave2 = (u32)__builtin_amdgcn_readfirstlane(opq(wave));
  const u32 vr = (lane >> 3) * 1280, ve = (lane >> 1) * 1280 + (lane & 1) * 16, vl = lane * 16;
  auto tile = [&](u32 v0, u32 v1, u32 r64, u32 cn) {
    const u32 so = r64 * 64 * 1280;
    dma4(L_XS, v0, v1 + 9216, v0 + 2 * 9216, v1 + 3 * 9216, rx, so + cn * 128);
    dma4(L_XS + 4096, v0, v1 + 9216, v0 + 2 * 9216, v1 + 3 * 9216, rx, so + cn * 128 + 32 * 1280);
    dma(L_XS + 8192, ve, rx, so + 1024 + cn * 32);
    dma(L_XS + 9216, ve, rx, so + 1024 + cn * 32 + 32 * 1280);
  };
  if (wave2 < 4) {
    __builtin_amdgcn_s_setprio(2);
    const u32 sym = SY_M + wave * 4;
    if (wave == 3) {
      u32x4 xr[10];
#pragma unroll
      for (int i = 0; i < 10; i++) asm volatile("buffer_load_dwordx4 %0, %1, %2, %3 offen offset:%4" : "=v"(xr[i]) : "v"(vr + (lane & 7) * 16), "s"(rx), "s"((pm * 128 + (pn ^ 8) * 8) * 1280), "i"(i * 128));
#pragma unroll
      for (int i = 0; i < 4; i++) {
        dma3(RB + 49152 + 3 * 12288 + i * 3072, vw[0], vw[1], vw[2], rw1, pn * 192 + (1 + 5 * rot1) * (64 * 3072) + i * 16 * 3072);
        dma4(RB + i * 4096, vx[0], vx[1], vx[2], vx[3], rx, (pm * 128 + i * 32) * 1280 + rot1 * 640 + 128);
      }
      dma_flag(SY_F + 4, rc);
      if (!odd) {
        const rsrc_t rw2 = mk(w2, 1536 * 1280);
        asm volatile("s_mov_b32 m0, %0\n\ts_nop 0\n\tbuffer_load_dword %1, %3, %4 offen lds\n\tbuffer_load_dword %2, %3, %4 offen lds"
                     ::"s"(L_DUMMY), "v"(vr + (lane & 7) * 128), "v"(lane < 32 ? vr + (lane & 7) * 128 + 8 * 1280 : OOR), "s"(rw2), "s"(pair * 12 * 1280));
      }
      asm volatile("s_waitcnt vmcnt(" VMX ")" : "+v"(xr[0]), "+v"(xr[1]), "+v"(xr[2]), "+v"(xr[3]), "+v"(xr[4]), "+v"(xr[5]), "+v"(xr[6]), "+v"(xr[7]), "+v"(xr[8]), "+v"(xr[9])::"memory");
      float s0 = 0.f, s1 = 0.f, q0 = 0.f, q1 = 0.f;
#pragma unroll
      for (int i = 0; i < 10; i++)
        asm("v_dot2_f32_bf16 %0, %4, %8, %0\n\tv_dot2_f32_bf16 %2, %4, %4, %2\n\tv_dot2_f32_bf16 %1, %5, %8, %1\n\tv_dot2_f32_bf16 %3, %5, %5, %3\n\t"
            "v_dot2_f32_bf16 %0, %6, %8, %0\n\tv_dot2_f32_bf16 %2, %6, %6, %2\n\tv_dot2_f32_bf16 %1, %7, %8, %1\n\tv_dot2_f32_bf16 %3, %7, %7, %3"
            : "+v"(s0), "+v"(s1), "+v"(q0), "+v"(q1) : "v"(xr[i].x), "v"(xr[i].y), "v"(xr[i].z), "v"(xr[i].w), "v"(0x3f803f80u));
      asm volatile("s_nop 3" : "+v"(s0), "+v"(s1), "+v"(q0), "+v"(q1));
      float sm = s0 + s1, sq = q0 + q1;
      sm = dpp_add<0xb1>(sm); sq = dpp_add<0xb1>(sq); sm = dpp_add<0x4e>(sm); sq = dpp_add<0x4e>(sq); sm = dpp_add<0x141>(sm); sq = dpp_add<0x141>(sq);
      const float mean = sm * (1.f / 640.f);
      const float var = rbf(fmaxf(__builtin_fmaf(-mean, mean, sq * (1.f / 640.f)), 0.f));
      const u32 pk = pk2(mean, __builtin_amdgcn_sqrtf(rbf(var + 1e-5f)));
      *lp<u32>(SY_RS + (lane >> 3) * 4) = pk;
    } else if (wave < nch) {
      f32x4 ka[2][2];
      kgen<1>(pw, ldt, wave == 0 ? RB + 16384 : RB + 49152 + (wave - 1) * 12288, lane, ka);
#pragma unroll
      for (int i = 0; i < 4; i++) *lp<f32x4>(L_SSM + wave * SSM_SZ + 512 + i * 1024 + vl) = ka[i >> 1][i & 1];
    }
    lds_set(SY_XF + wave * 4, 1);
    STAMP(1);
    const u32 h = lane >> 5;
    u32 ax[4];
#pragma unroll
    for (int ks = 0; ks < 4; ks++) ax[ks] = opq(ldx + (lane & 31) * 128 + (((2 * ks + h) ^ ((lane >> 1) & 7)) << 4));
    const u32 aw = opq(RB + 49152 + (8 * h + tq) * 192 + (gam & 1) * 32 + tp * 8);
    f32x16 acc[3];
#pragma unroll
    for (int ft = 0; ft < 3; ft++)
#pragma unroll
      for (int i = 0; i < 16; i++) acc[ft][i] = 0.f;
    u32x4 Xf[2][4];
    u32x2 Wf[2][4][3][2];
    auto rd = [&](int s, int i) {
      const int ks = i / 7, r = i % 7;
      if (r == 6) Xf[s & 1][ks] = *lp<u32x4>(ax[ks] + ((s + 2) % 3) * 16384);
      else Wf[s & 1][ks][r >> 1][r & 1] = trd(aw + ((s + 2) % 4) * 12288 + (ks * 16 + (r & 1) * 4) * 192 + (r >> 1) * 64);
    };
    auto mm1 = [&](int s, int i) {
      const int ks = s < 9 ? i / 3 : i % 4, ft = s < 9 ? i % 3 : i / 4;
      acc[ft] = mf32(frag(Wf[s & 1][ks][ft][0], Wf[s & 1][ks][ft][1]), fragv(Xf[s & 1][ks]), acc[ft]);
    };
    chk4(ldflags4(SY_F0), SY_F0, 1);
    chk1(ldflag(SY_XF), SY_XF, 1);
    STAMP(2);
#pragma unroll
    for (int i = 0; i < 28; i++) { rd(0, i); if (i % 7 == 6) pieceX(i / 7, 2); }
    u32 fl = ldflag(SY_F + 4);
    lds_set(sym, 1);
    __builtin_amdgcn_s_setprio(M_PRIO);
    const rsrc_t rw2 = mk(w2, 1536 * 1280);
    const u32 v2 = (lane >> 3) * 1280 + (((lane & 7) ^ (((lane >> 4) & 3) << 1)) << 4);
    const u32 so2b = ((wave >> 1) * 768 + (wave & 1) * 32) * 1280 + qn * 128;
    const u32 so2x = WS_W2X + qn * 49152 + ((wave >> 1) * 768 + (wave & 1) * 32) * 32;
    f32x4 cs[12], bb[12];
    f32x2 st;
    sfor<10>([&](auto S_) {
      constexpr int s = S_;
      if (s == 9) STAMP(4);
      if (s == 4) STAMP(11);
      chk1(fl, s < 9 ? SY_F + (s + 1) * 4 : SY_EPI1, 1);
      if (s > 0 && s < 9) VMCNT(4);
      SB();
#pragma unroll
      for (int gi = 0; gi < 12; gi++) {
        mm1(s, gi);
        if (gi < 8) {
          if (s < 9) {
#pragma unroll
            for (int i = (gi * 7) / 2; i < ((gi + 1) * 7) / 2; i++) rd(s + 1, i);
          } else {
            const u32 cb = opq(L_CS + h * 16);
            if (gi == 0) st = *lp<f32x2>(L_ST + (wave * 32 + (lane & 31)) * 8);
#pragma unroll
            for (int i = gi * 3; i < gi * 3 + 3; i++) { if (i & 1) bb[i >> 1] = *lp<f32x4>(cb + 384 + (i >> 1) * 32); else cs[i >> 1] = *lp<f32x4>(cb + (i >> 1) * 32); }
          }
        }
        if (gi < 4) {
          if (s < 7) pieceX(gi, s + 3);
          else dma(ldx + (s - 7) * 16384 + gi * 1024, v2, rw2, so2b + (ck(s - 7) * 64 + gi * 8) * 1280);
        }
        if (gi == 9 && s == 9) dma_flag(SY_F + 64 + wave * 4, rc);
        if (gi == 7 && s < 9) lds_set(sym, s + 2);
        if (gi == 9 && s < 9) fl = ldflag(s < 8 ? SY_F + (s + 2) * 4 : SY_EPI1);
        SB();
      }
    });
    __builtin_amdgcn_s_setprio(2);
    STAMP(3);
    {
      const u32 vo = WS_ACT + ((pm * 4 + wave) * 48 + pn * 3) * 2048 + lane * 16;
#pragma unroll
      for (int it = 0; it < 6; it++) {
        const int ft = it >> 1, eta = it & 1;
        u32x4 o;
#pragma unroll
        for (int p = 0; p < 4; p++) {
          const int ci = ft * 4 + eta * 2 + (p >> 1), j = (p & 1) * 2;
          const f32x2 two = {2.f, 2.f}, one = {1.f, 1.f};
          f32x2 hh = __builtin_elementwise_fma(st.xx, f32x2{acc[ft][eta * 8 + 2 * p], acc[ft][eta * 8 + 2 * p + 1]},
                                               __builtin_elementwise_fma(st.yy, f32x2{cs[ci][j], cs[ci][j + 1]}, f32x2{bb[ci][j], bb[ci][j + 1]}));
          f32x2 e = {__builtin_amdgcn_exp2f(hh.x), __builtin_amdgcn_exp2f(hh.y)};
          f32x2 dd = __builtin_elementwise_fma(e, e + two, two);
          f32x2 rr = {__builtin_amdgcn_rcpf(dd.x), __builtin_amdgcn_rcpf(dd.y)};
          f32x2 ac = hh * __builtin_elementwise_fma(-two, rr, one);
          o[p] = pk2(ac.x, ac.y);
        }
        st16c(o, rws, vo, it * 1024);
      }
    }
    lds_set(sym, 13);
    const u32x2 tag = *lp<u32x2>(SY_TAG);
    STAMP(5);
    __builtin_amdgcn_s_setprio(3);
    const u32 m2 = wave & 1, kh = wave >> 1;
    const u32 eta2 = gam >> 1, h2 = gam & 1;
    u32 bw[4], bx;
    {
      const u32 row = eta2 * 16 + h2 * 4 + tq, sg = (2 * h2 + (tq >> 1)) & 3;
#pragma unroll
      for (int nt = 0; nt < 4; nt++) bw[nt] = opq(RB + kh * 8192 + row * 128 + ((nt ^ sg) << 5) + tp * 8);
      bx = opq(RB + 81920 + kh * 2048 + row * 32 + tp * 8);
    }
    u32x2 W2[24][5][2];
    u32 bt[5];
    auto rd2 = [&](int k, int i) {
      const int c = k >> 1, sl = c < 5 ? c : c < 8 ? c - 5 : c - 7, kk = k & 1, nt = i >> 1, t = i & 1;
      const u32 in = nt < 4 ? kk * 4096 + t * 1024 : kk * 1024 + t * 256;
      if (c == 8 || c == 9) {
        if (k == 16 && t == 0) bt[nt] = opq(nt < 4 ? bw[nt] - RB : bx - RB - 81920 + 32768);
        W2[k][nt][t] = trd(bt[nt] + (c - 8) * (nt < 4 ? 16384 : 4096) + in);
      } else if (sl == 4 && nt < 4) {
        if (kk == 0 && t == 0) bt[nt] = opq(bw[nt] + 65536);
        W2[k][nt][t] = trd(bt[nt] + in);
      } else W2[k][nt][t] = trd(nt < 4 ? bw[nt] + sl * 16384 + in : bx + sl * 4096 + in);
    };
    chk4(ldflags4(SY_F + 64), SY_F + 64, 1);
    chk1(ldflag(SY_F + 44), SY_F + 44, 1);
#pragma unroll
    for (int i = 0; i < 60; i++) rd2(i / 10, i % 10);
    lds_set(sym, 14);
    publish<1>(rws, WS_FB + ((pm * 4 + wave) * 16 + pn) * 64, tag, lane);
    STAMP(6);
    __builtin_amdgcn_s_setprio(0);
    const u32 rl = m2 * 32 + kh * 16;
    { u32 po[1] = {lane < 8 ? lane * 64 : OOR}; __builtin_amdgcn_s_sleep(B_DELAY); poll_flags<1, B_SLEEP>(rws, po, WS_FB + ((qm * 2 + m2) * 16 + kh * 8) * 64, tag); }
    STAMP(7);
    const u32 va = eta2 * 1024 + (h2 * 32 + c16) * 16 + kh * 256, vb = va ^ 256;
    const u32 soa0 = WS_ACT + ((qm * 2 + m2) * 48 + kh * 24) * 2048;
    const u32 soa = soa0 + rot * 12 * 2048, soa2 = soa0 - rot * 12 * 2048;
    constexpr int WIN = G2_WIN;
    u32x4 af[32][2];
#pragma unroll
    for (int k = 0; k < WIN; k++) { af[k][0] = ld16c(rws, va, soa + k * 2048); af[k][1] = ld16c(rws, vb, soa + k * 2048); }
    f32x4 c2[5][2];
#pragma unroll
    for (int nt = 0; nt < 5; nt++) { c2[nt][0] = f32x4{0, 0, 0, 0}; c2[nt][1] = f32x4{0, 0, 0, 0}; }
    SB();
    u32 ff = 0;
    u32 e_xo, e_xe, e_rb, e_rp;
    u32x2 sv[5], xv[5];
    float sf[20], xf[20];
    sfor<24>([&](auto K_) {
      constexpr int k = K_;
      constexpr int kr = k < 4 ? k + 6 : k >= 8 && k < 22 ? k + 2 : -1;
      if (k == 7 || k == 13 || k == 17) ff = ldflag(k == 7 ? SY_F + 48 : k == 13 ? SY_FULL : SY_F + 52);
      if (k == 8 || k == 14 || k == 18) chk1(ff, k == 8 ? SY_F + 48 : k == 14 ? SY_FULL : SY_F + 52, 1);
      if (k == 21) ff = ldflag(SY_EPI2);
      if (k == 22) {
        STAMP(8);
        chk1(ff, SY_EPI2, 1);
        STAMP(9);
        const u32 ln = __builtin_amdgcn_mbcnt_hi(~0u, __builtin_amdgcn_mbcnt_lo(~0u, opq(0u)));
        const u32 c16 = ln & 15, gam = ln >> 4, tq = (ln >> 2) & 3;
        const u32 e_sb = opq(L_SIMG + (gam * 4 + tq) * 128 + ((((rl >> 4) ^ (tq >> 1) ^ ((gam & 1) << 1)) & 3) << 5) + (ln & 3) * 8);
        e_xo = L_XS + (rl + c16) * 128 + (((gam >> 1) ^ (c16 & 7)) << 4) + (gam & 1) * 8;
        e_xe = L_XS + 8192 + (rl + c16) * 32 + gam * 8;
        e_rb = opq(L_RED + kh * 16384 + m2 * 5120 + ln * 16); e_rp = opq(L_RED + (kh ^ 1) * 16384 + m2 * 5120 + ln * 16);
#pragma unroll
        for (int nt = 0; nt < 5; nt++) { sv[nt] = trd(e_sb + nt * 2048); xv[nt] = *lp<u32x2>(nt < 4 ? ((e_xo - L_XS) ^ (nt * 32)) + L_XS : e_xe); }
      }
      SB();
#pragma unroll
      for (int j = 0; j < 10; j++) {
        const int i = k < 23 ? j : j < 5 ? 2 * j + 1 : 2 * (j - 5);
        c2[i >> 1][i & 1] = mf16(frag(W2[k][i >> 1][0], W2[k][i >> 1][1]), fragv(af[k][i & 1]), c2[i >> 1][i & 1]);
        if (kr >= 0) { if (k >= 8) rd2(kr, i); else if (i >= 2) { rd2(kr, i - 2); if (i >= 8) rd2(kr, i); } }
        if (k >= 1 && k - 1 + WIN < 24 && (i == 0 || i == 5)) af[k - 1 + WIN][i / 5] = ld16c(rws, i ? vb : va, (k - 1 + WIN < 12 ? soa : soa2) + (k - 1 + WIN) * 2048);
        if (k == 23 && j >= 5) *lp<f32x4>(e_rb + (j - 5) * 1024) = c2[j - 5][1];
        SB();
      }
      if (k == 3) lds_set(sym, 16);
      if (k == 9) lds_set(sym, 17);
      if (k == 13) lds_set(sym, 18);
    });
    lds_set(SY_RED + wave * 4, 1);
#pragma unroll
    for (int i = 0; i < 10; i++) {
      sf[2 * i] = bflo(sv[i >> 1][i & 1]); sf[2 * i + 1] = bfhi(sv[i >> 1][i & 1]); xf[2 * i] = bflo(xv[i >> 1][i & 1]); xf[2 * i + 1] = bfhi(xv[i >> 1][i & 1]);
      asm volatile("" : "+v"(sf[2 * i]), "+v"(sf[2 * i + 1]), "+v"(xf[2 * i]), "+v"(xf[2 * i + 1]));
      if (i == 3) ff = ldflag(SY_RED + (wave ^ 2) * 4);
    }
    chk1(ff, SY_RED + (wave ^ 2) * 4, 1);
    {
      f32x4 pv[5];
#pragma unroll
      for (int nt = 0; nt < 5; nt++) pv[nt] = *lp<f32x4>(e_rp + nt * 1024);
      const u32 ln = __builtin_amdgcn_mbcnt_hi(~0u, __builtin_amdgcn_mbcnt_lo(~0u, opq(0u)));
      const u32 e_ob = opq(L_XS + (rl + (ln >> 3)) * 128 + (((ln & 7) ^ (ln >> 3)) << 4)), e_oe = opq(L_XS + 8192 + rl * 32 + (ln & 31) * 16);
      const u32 e_vo = opq((qm * 64 + rl + (ln >> 3)) * 1280 + (ln & 7) * 16 + qn * 128), e_vo2 = opq(e_vo + 8 * 1280);
      const u32 e_ve = opq(ln < 32 ? (qm * 64 + rl + (ln >> 1)) * 1280 + (ln & 1) * 16 + 1024 + qn * 32 : OOR);
      SB();
#pragma unroll
      for (int nt = 0; nt < 5; nt++) {
        u32x2 r;
#pragma unroll
        for (int j = 0; j < 2; j++) {
          const int e = nt * 4 + j * 2;
          const f32x2 o = f32x2{pv[nt][2 * j], pv[nt][2 * j + 1]} + f32x2{c2[nt][0][2 * j], c2[nt][0][2 * j + 1]};
          const f32x2 dn = f32x2{__builtin_amdgcn_exp2f(-o.x), __builtin_amdgcn_exp2f(-o.y)} + f32x2{1.f, 1.f};
          const f32x2 rc_ = {__builtin_amdgcn_rcpf(dn.x), __builtin_amdgcn_rcpf(dn.y)};
          const f32x2 y = __builtin_elementwise_fma(o * f32x2{sf[e], sf[e + 1]}, rc_, f32x2{xf[e], xf[e + 1]});
          r[j] = pk2(y.x, y.y);
        }
        *lp<u32x2>(nt < 4 ? ((e_xo - L_XS) ^ (nt * 32)) + L_XS : e_xe) = r;
      }
      CFENCE();
      const rsrc_t ro = mk(out, 2048 * 1280);
      const u32x4 v0 = *lp<u32x4>(e_ob), v1 = *lp<u32x4>(e_ob + 1024), v2_ = *lp<u32x4>(e_oe);
      st16(v0, ro, e_vo, 0);
      st16(v1, ro, e_vo2, 0);
      st16(v2_, ro, e_ve, 0);
    }
    STAMP(10);
  } else if (wave2 == 7) {
    __builtin_amdgcn_s_setprio(3);
    const rsrc_t rw2 = mk(w2, 1536 * 1280);
    u32 v2[4];
    auto ldW = [&](u32 s) {
      const u32 so = pn * 192 + (s < 5 ? s + 5 * rot1 : s - 5 * rot1) * (64 * 3072), ld = RB + 49152 + ((s + 2) & 3) * 12288;
#pragma unroll
      for (int i = 0; i < 4; i++) dma3(ld + i * 3072, vw[0], vw[1], vw[2], rw1, so + i * 16 * 3072);
      dma_flag(SY_F + s * 4, rc);
    };
    auto ld2q = [&](u32 j, u32 slot, u32 q) {
      dma4(RB + slot * 16384 + q * 4096, v2[0], v2[1], v2[2], v2[3], rw2, ((q >> 1) * 768 + ck(j) * 64 + (q & 1) * 32) * 1280 + qn * 128);
    };
    auto ld2x = [&](u32 j, u32 slot) {
      dma2c(RB + 81920 + slot * 4096, vl, rws, WS_W2X + qn * 49152 + ck(j) * 2048);
      dma2c(RB + 81920 + slot * 4096 + 2048, vl, rws, WS_W2X + qn * 49152 + 768 * 32 + ck(j) * 2048);
    };
    { const u32 vt = vr + (((lane & 7) ^ (((lane >> 4) & 1) << 1)) << 4); tile(vt, vt ^ 64, tm, tn); }
    const u32x2 ep = ld8c(rws, 0, WS_EPOCH);
    SB();
    VMCNT(1);
    STAMP(9);
    {
      u32x4 t[10];
      const u32 sg = (tq >> 1) | ((gam & 1) << 1);
      const u32 ra = L_XS + (gam * 8 + tq) * 128 + tp * 8, re = opq(L_XS + 8192 + (gam * 8 + tq) * 32 + tp * 8);
#pragma unroll
      for (int cb = 0; cb < 5; cb++) {
        const u32 a = cb < 4 ? opq(ra + ((cb ^ sg) << 5)) : re;
#pragma unroll
        for (int hf = 0; hf < 2; hf++) {
          u32x2 a0 = trd(a + hf * (cb < 4 ? 4096 : 1024)), a1 = trd(a + hf * (cb < 4 ? 4096 : 1024) + (cb < 4 ? 512 : 128));
          t[cb * 2 + hf] = u32x4{a0.x, a0.y, a1.x, a1.y};
        }
      }
      CFENCE(); SB();
      const u32 wa0 = opq(L_XS + c16 * 128 + ((gam ^ (c16 & 7)) << 4)), wa1 = opq(wa0 ^ 64);
#pragma unroll
      for (int cb = 0; cb < 5; cb++) { *lp<u32x4>(wa0 + cb * 2048) = t[cb * 2]; *lp<u32x4>(wa1 + cb * 2048) = t[cb * 2 + 1]; }
      *lp<u32x4>(SY_TAG) = u32x4{ep.x + 1, ep.y + 0x9e3779b9u, 1u, 0u};
      CFENCE(); SB();
      const u32 rb = opq(L_XS + (lane >> 3) * 128 + (((lane & 7) ^ (lane >> 3)) << 4));
#pragma unroll
      for (int i = 0; i < 10; i++) t[i] = *lp<u32x4>(rb + i * 1024);
      SB();
      const u32 vo = (lane >> 3) * 4096 + (lane & 7) * 16 + WS_XT + tm * 128;
#pragma unroll
      for (int i = 0; i < 10; i++) st16c(t[i], rws, vo, (i < 8 ? tn * 64 + i * 8 : 512 + tn * 16 + (i - 8) * 8) * 4096);
      SB();
    }
    STAMP(10);
    lds_spin4(SY_XF, 1);
    st16c(*lp<u32x4>(SY_RS + (lane & 1) * 16), rws, lane < 2 ? WS_STG + lane * 4096 + (pm * 16 + (pn ^ 8)) * 16 : OOR, 0);
    {
      const u32x2 rs = *lp<u32x2>(SY_RS + (lane & 3) * 8);
      st16c(u32x4{rs.x, ep.x + 1, rs.y, ep.y + 0x9e3779b9u}, rws, lane < 4 ? WS_REC + (pm * 16 + (pn ^ 8)) * 128 + lane * 16 : OOR, 0);
    }
    SB();
    ldW(2); ldW(3);
    VMCNT(26);
    st8c(u32x2{ep.x + 1, ep.y + 0x9e3779b9u}, rws, lane == 0 ? 0 : OOR, WS_F1 + g8 * REP + r5 * 64);
    STAMP(1);
#pragma unroll
    for (int j = 0; j < 4; j++) v2[j] = vr + (((lane & 7) ^ (((lane >> 4) & 3) << 1)) << 4) + j * 9216;
#pragma unroll 1
    for (u32 s = 4; s < 10; s++) { lds_spin4(SY_M, s - 3); ldW(s); }
    STAMP(5);
    {
      const u32x2 tag = {ep.x + 1, ep.y + 0x9e3779b9u};
      const u32x2 bv = *(const u32x2*)(b1 + pn * 96 + min(lane, 23) * 4);
      u32x4 g[6];
#pragma nounroll
      for (u32 spin = 0; spin < SPIN_CAP; spin++) {
        CFENCE();
        g[0] = ld16c(rws, (lane >> 2) * 128 + (lane & 3) * 16, WS_REC + pm * 2048);
#pragma unroll
        for (int k = 0; k < 5; k++) g[1 + k] = ld16c(rws, min(lane, 47) * 16, WS_CSP + k * 12288 + pn * 768);
        bool ok = true;
#pragma unroll
        for (int k = 0; k < 6; k++) ok = ok && g[k].y == tag.x && g[k].w == tag.y;
        if (__builtin_amdgcn_ballot_w64(!ok) == 0) break;
        __builtin_amdgcn_s_sleep(8);
      }
      CFENCE();
      STAMP(8);
      const float r0 = LOG2E * __builtin_amdgcn_rcpf(bfhi(g[0].x)), r1 = LOG2E * __builtin_amdgcn_rcpf(bfhi(g[0].z));
      *lp<f32x4>(L_ST + lane * 16) = f32x4{r0, -bflo(g[0].x) * r0, r1, -bflo(g[0].z) * r1};
      f32x2 cs = {0.f, 0.f};
#pragma unroll
      for (int k = 0; k < 5; k++) cs += f32x2{__uint_as_float(g[1 + k].x), __uint_as_float(g[1 + k].z)};
      if (lane < 48) *lp<f32x2>(L_CS + lane * 8) = cs;
      if (lane < 24) *lp<f32x4>(L_B1 + lane * 16) = f32x4{bflo(bv.x) * LOG2E, bfhi(bv.x) * LOG2E, bflo(bv.y) * LOG2E, bfhi(bv.y) * LOG2E};
      lds_set(SY_EPI1, 1);
    }
    { const u32 vs = vr + (((lane & 7) ^ (lane >> 3)) << 4); tile(vs, vs, qm, qn); }
    STAMP(2);
    lds_spin4(SY_M, 7);
    ld2q(3, 3, 0); ld2q(3, 3, 1); ld2q(3, 3, 2);
    lds_spin4(SY_M, 8);
    ld2q(3, 3, 3); ld2q(4, 4, 0); ld2q(4, 4, 1);
    lds_spin4(SY_M, 9);
    ld2q(4, 4, 2); ld2q(4, 4, 3);
    lds_spin4(SY_M, 10);
    lds_wait(SY_W2X, 1);
#pragma unroll 1
    for (u32 j = 0; j < 5; j++) ld2x(j, j);
    dma_flag(SY_F + 44, rc);
    STAMP(3);
    u32x4 pk_[2][20];
#pragma unroll
    for (int j = 0; j < 2; j++)
#pragma unroll
      for (int i = 0; i < 20; i++) {
        if (i < 16) pk_[j][i] = ld16(rw2, v2[0], ((i >> 3) * 768 + ck(8 + j) * 64 + (i & 7) * 8) * 1280 + qn * 128);
        else pk_[j][i] = ld16c(rws, vl, WS_W2X + qn * 49152 + (((i - 16) >> 1) * 768 + ck(8 + j) * 64) * 32 + (i & 1) * 1024);
      }
    SB();
    STAMP(4);
    lds_spin4(SY_M, 14);
#pragma unroll 1
    for (u32 j = 0; j < 3; j++) {
#pragma unroll
      for (int q = 0; q < 4; q++) ld2q(5 + j, j, q);
      ld2x(5 + j, j);
    }
    dma_flag(SY_F + 48, rc);
    lds_wait4(SY_SDONE, 1);
    const u32 wb = opq(L_SSM + vl);
#pragma unroll
    for (int j = 0; j < 2; j++)
#pragma unroll
      for (int i = 0; i < 20; i++) *lp<u32x4>(wb + (i < 16 ? j * 16384 + i * 1024 : 32768 + j * 4096 + (i - 16) * 1024)) = pk_[j][i];
    lds_set(SY_FULL, 1);
    STAMP(6);
    lds_wait4(SY_M, 16);
#pragma unroll 1
    for (u32 j = 0; j < 2; j++) {
#pragma unroll
      for (int q = 0; q < 4; q++) ld2q(10 + j, 3 + j, q);
      ld2x(10 + j, 3 + j);
    }
    dma_flag(SY_F + 52, rc);
    VMCNT(0);
    STAMP(7);
  } else if (wave2 - 4 < nch) {
    const u32 LU = L_SSM + (wave - 4) * SSM_SZ, LB = LU + 4608;
    __builtin_amdgcn_s_setprio(S_PRIO);
    STAMP(1);
    f32x4 ka[2][2];
    kgen<2>(pw, ldt, LB, lane, ka);
    STAMP(2);
    const u32 fk = ldflag(SY_XF + (wave - 4) * 4);
    u32x4 tb[8];
    {
      const u32 dum = LB + LB_DUM + lane * 8;
      const bool g2 = gam == 2, g3 = gam == 3, lo3 = g3 && c16 < 2;
      const u32 kb = LB + 32 + 16 * c16 + (gam < 2 ? 8 * gam : g2 ? 512 : 1024), t4 = LB + T4 + 116 + 4 * c16;
      tb[4] = u32x4{(!g3 || lo3) ? kb : dum, (g2 || lo3) ? kb + 8 : g3 ? t4 : dum, g2 ? kb + 12 : g3 ? t4 + 128 : dum, ((g2 && c16 > 0) || lo3) ? kb - 2 : dum};
      tb[5] = u32x4{!g3 ? kb + 256 : dum, g2 ? kb + 264 : g3 ? t4 + 64 : dum, g2 ? kb + 268 : g3 ? t4 + 192 : dum, g2 ? kb + 254 : dum};
      tb[6] = u32x4{gam < 2 ? 0x03020100u : 0x01000100u, gam < 2 ? 0x07060504u : g2 ? 0x05040302u : 0x03020100u,
                    g2 ? 0x05040504u : c16 < 2 ? 0x03020302u : 0x03020100u, g2 ? 0x07060706u : 0x07060504u};
      tb[7] = u32x4{g2 ? 0x05040504u : 0x03020100u,
                    lane == 0 ? LB + (16 + 518) * 2 : lane == 1 ? LB + T4 + 186 * 2 : lane < 11 ? LB + (16 + 577 + lane) * 2 : lane < 13 ? LB + T8 + (18 + lane) * 2 : dum,
                    0u, LB + lane * 16 + (lane < 2 ? 0 : 1024)};
      asm volatile("" : "+v"(tb[4]), "+v"(tb[5]), "+v"(tb[6]), "+v"(tb[7]));
      SB();
    }
    if (lane < 32) *lp<u32x4>(LU + lane * 16) = u32x4{0, 0, 0, 0};
    const u32 lb16 = opq(LB + lane * 16);
    *lp<u32x4>(tb[7].w) = u32x4{0, 0, 0, 0};
    *lp<u32x4>(lb16 + T4) = u32x4{0, 0, 0, 0};
    CFENCE();
    chk1(fk, SY_XF + (wave - 4) * 4, 1);
#pragma unroll
    for (int i = 0; i < 4; i++) ka[i >> 1][i & 1] += *lp<f32x4>(LU + 512 + i * 1024 + vl);
    lds_set(SY_KD + (wave - 4) * 4, 1);
    if (wave == 4) lds_set(SY_KD + 12, 1);
    if (lane == 0) ka[0][0][0] += bflo(dsk) * LN2;
    {
      u32 a01[2], a23[2], b01[2], b23[2];
#pragma unroll
      for (int qb = 0; qb < 2; qb++) {
        a01[qb] = pk2(ka[0][qb][0], ka[0][qb][1]); a23[qb] = pk2(ka[0][qb][2], ka[0][qb][3]);
        b01[qb] = pk2(ka[1][qb][0], ka[1][qb][1]); b23[qb] = pk2(ka[1][qb][2], ka[1][qb][3]);
      }
      const u32 t8a = opq(LB + T8 + (29 + c16 + 128 * gam) * 2);
#pragma unroll
      for (int qb = 0; qb < 2; qb++) {
        *lp<u32x2>(tb[4 + qb].x) = u32x2{__builtin_amdgcn_perm(a23[qb], a01[qb], tb[6].x), __builtin_amdgcn_perm(a23[qb], a01[qb], tb[6].y)};
        *lp<u32>(tb[4 + qb].y) = __builtin_amdgcn_perm(a23[qb], a01[qb], qb ? tb[7].x : tb[6].z);
        *lp<u32>(tb[4 + qb].z) = __builtin_amdgcn_perm(a23[qb], a01[qb], tb[6].w);
        *lp<u16>(t8a + qb * 32) = (u16)b01[qb];
        *lp<u16>(t8a + qb * 32 + 64) = (u16)(b01[qb] >> 16);
        *lp<u16>(t8a + qb * 32 + 128) = (u16)b23[qb];
        *lp<u16>(t8a + qb * 32 + 192) = (u16)(b23[qb] >> 16);
      }
      CFENCE();
      *lp<u16>(tb[4].w) = (u16)a01[0];
      *lp<u16>(tb[5].w) = (u16)a01[1];
      const u32 k520 = __builtin_amdgcn_readlane(a01[0], 49), k1024 = __builtin_amdgcn_readlane(b01[0], 0), k1032 = __builtin_amdgcn_readlane(b01[0], 1);
      *lp<u16>(tb[7].y) = (u16)(lane == 0 ? k520 : lane < 4 ? k1024 : lane < 11 ? k1032 : 0u);
    }
    CFENCE();
    {
      const u32 lb2 = opq(LB + (64 + min((u32)lane, 13u)) * 16);
      u32x4 wv[2]; u32x2 yv[2];
      wv[0] = *lp<u32x4>(lb16); yv[0] = *lp<u32x2>(lb16 - 8); wv[1] = *lp<u32x4>(lb2); yv[1] = *lp<u32x2>(lb2 - 8);
      SB();
#pragma unroll
      for (int ps = 0; ps < 2; ps++) {
        const u32 b = ps ? lb2 : lb16;
        const u32x4 a = wv[ps]; const u32x2 y = yv[ps];
        const u32 s0 = __builtin_amdgcn_alignbit(y.y, y.x, 16), s1 = __builtin_amdgcn_alignbit(a.x, y.y, 16), s2 = __builtin_amdgcn_alignbit(a.y, a.x, 16),
                  s3 = __builtin_amdgcn_alignbit(a.z, a.y, 16), s4 = __builtin_amdgcn_alignbit(a.w, a.z, 16);
        *lp<u32x4>(b + 1344) = u32x4{s1, s2, s3, s4};
        *lp<u32x4>(b + 2688) = u32x4{y.y, a.x, a.y, a.z};
        *lp<u32x4>(b + 4032) = u32x4{s0, s1, s2, s3};
      }
      CFENCE();
    }
    bf16x8 Ff[18], H4[6], H8[4];
    {
      const u32 ha = opq(LB + T4 + (4 * c16 + 8 * gam) * 2), ha2 = opq(ha + 8);
#pragma unroll
      for (int e = 0; e < 6; e++) H4[e] = frag(*(volatile LAS u32x2*)lp<u32x2>(ha + e * 64), *(volatile LAS u32x2*)lp<u32x2>(ha2 + e * 64));
      const u32 hb = opq(LB + T8 + (2 * c16 + 8 * gam) * 2);
#pragma unroll
      for (int e = 0; e < 4; e++) {
        u32x4 v;
#pragma unroll
        for (int j = 0; j < 4; j++) v[j] = *(volatile LAS u32*)lp<u32>(hb + e * 64 + j * 4);
        H8[e] = fragv(v);
      }
      const u32 fa = opq(LB + tq * 1344 + (28 - 8 * gam + 4 * tp) * 2);
#pragma unroll
      for (int m = 0; m < 18; m++) { const int mm = m < 17 ? m : 18; Ff[m] = frag(trd(fa + mm * 64 + 8), trd(fa + mm * 64)); }
    }
    CFENCE();
    *lp<u32x4>(LB + (lane == 0 ? 0 : lane < 9 ? S5 + 3072 - 16 : lane < 16 ? S3 + 3072 - 144 : lane < 18 ? S9 + 2048 - 256 : S7 + 2048 - 288) + min(lane, 19) * 16) = u32x4{0, 0, 0, 0};
#pragma unroll
    for (int wd = 0; wd < 4; wd++) {
      const u32 msk = ((((1u << (2 * wd + 3)) - 1u) << (c16 + 7 - wd)) >> (8 * gam)) & 255u, m2 = msk | (msk << 15);
#pragma unroll
      for (int j = 0; j < 4; j++) tb[wd][j] = __umul24((m2 >> (2 * j)) & 0x10001u, 0x3f80u);
      asm volatile("" : "+v"(tb[wd]));
    }
    STAMP(3);
    if (wave < 6) {
      u32x4 sa[2][2], xa[3][2];
      if (wave == 4) {
        u32 po[1] = {lane < 8 ? lane * 128 : OOR};
        const u32x2 tg = get_tag();
        poll_flags<1, A_SLEEP>(rws, po, WS_FA + g8 * REP, tg);
        lds_set(SY_W2X, 1);
        if (w == 0) st8c(tg, rws, lane == 0 ? 0 : OOR, WS_EPOCH);
      } else lds_wait(SY_W2X, 1);
      lds_wait4(SY_KD, 1);
      STAMP(10);
      __builtin_amdgcn_s_setprio(3);
#pragma unroll
      for (int i = 0; i < 2; i++) {
#pragma unroll
        for (int j = 0; j < 2; j++) sa[i][j] = ld16c(rws, lane * 16, WS_STG + j * 4096 + ((wave - 4) * 2 + i) * 1024);
#pragma unroll
        for (int c = 0; c < 3; c++) xa[c][i] = ld16c(rws, lane * 16, WS_XT + min(ch0 + c, 639u) * 4096 + ((wave - 4) * 2 + i) * 1024);
      }
      SB();
      __builtin_amdgcn_s_setprio(S_PRIO);
      const u32 ub = opq(L_SSM + 512 + (wave - 4) * 2048 + lane * 16);
#pragma unroll
      for (int i = 0; i < 2; i++) {
        u32x4 o[3];
#pragma unroll
        for (int j = 0; j < 4; j++) {
          const u32 p0 = sa[i][j >> 1][(j & 1) * 2], p1 = sa[i][j >> 1][(j & 1) * 2 + 1];
          const float r0 = __builtin_amdgcn_rcpf(bfhi(p0)), r1 = __builtin_amdgcn_rcpf(bfhi(p1));
          const float m0 = -bflo(p0) * r0, m1 = __uint_as_float(opq(__float_as_uint(-bflo(p1) * r1)));
#pragma unroll
          for (int c = 0; c < 3; c++) {
            float u0;
            asm("v_fma_f32 %0, %1, %2, %3" : "=v"(u0) : "v"(bflo(xa[c][i][j])), "v"(r0), "v"(m0));
            o[c][j] = pk2(u0, __builtin_fmaf(bfhi(xa[c][i][j]), r1, m1));
          }
        }
#pragma unroll
        for (int c = 0; c < 3; c++) *lp<u32x4>(ub + c * SSM_SZ + i * 1024) = o[c];
      }
      *lp<u32x2>(SY_U + (wave - 4) * 8) = u32x2{1u, 1u};
      CFENCE();
      STAMP(11);
    }
    lds_wait4(SY_U, 1);
    STAMP(7);
    const u32 ub = opq(LU + 32 + 32 * c16 + 16 * gam);
    const u32 g4 = opq(LB + ((tq & 1) ? S3 : S5) - 8 + (108 - 32 * gam - 4 * tq + 4 * tp) * 2);
    const u32 g8a = opq(LB + ((tq & 1) ? S9 : S7) + (216 - 64 * gam - 8 * tq + 4 * tp) * 2);
    const u32 sb = opq(LB + 32 * c16 + 8 * gam);
    constexpr int RING = 8, AHEAD = 6;
    u32x4 uf[RING];
    auto ldf = [&](int n) {
      if (n < 12) { u32x2 a = trd(g4 + n * 256 + 32), b = trd(g4 + n * 256); uf[n % RING] = u32x4{a.x, a.y, b.x, b.y}; }
      else if (n < 16) { u32x2 a = trd(g8a + (n - 12) * 512 + 64), b = trd(g8a + (n - 12) * 512); uf[n % RING] = u32x4{a.x, a.y, b.x, b.y}; }
      else if (n < 80) uf[n % RING] = *lp<u32x4>(ub + (n - 16) * 64);
    };
    {
      u32x4 sf[6];
#pragma unroll
      for (int t = 0; t < 6; t++) sf[t] = *lp<u32x4>(ub + 432 + t * 512);
      SB();
      f32x4 sr_[2][4];
#pragma unroll
      for (int t = 0; t <= 6; t++) {
#pragma unroll
        for (int wd = 0; wd < 4; wd++) {
          if (t < 6 && wd < (t < 4 ? 4 : 2)) sr_[t & 1][wd] = mf16(fragv(tb[wd]), fragv(sf[t]), f32x4{0, 0, 0, 0});
          if (t > 0 && wd < (t - 1 < 4 ? 4 : 2)) {
            const f32x4 s = sr_[(t - 1) & 1][wd];
            *lp<u32x2>(sb + (wd == 0 ? S3 : wd == 1 ? S5 : wd == 2 ? S7 : S9) + (t - 1) * 512) = u32x2{pk2(s[0], s[1]), pk2(s[2], s[3])};
          }
          SB();
        }
      }
    }
    CFENCE();
#pragma unroll
    for (int n = 0; n < AHEAD; n++) ldf(n);
    f32x4 y[8];
#pragma unroll
    for (int c = 0; c < 8; c++) y[c] = f32x4{0, 0, 0, 0};
    const u32 yr = opq(LB + vl), yo = opq(vl + WS_ST + d * 4096);
    const u32x2 tag = *lp<u32x2>(SY_TAG);
    u32x4 ov;
    sfor<88>([&](auto N) {
      constexpr int n = N;
      if (n == YIELD_AT) {
        lds_wait(SY_M + (wave - 4) * 4, 13);
        __builtin_amdgcn_s_setprio(2);
        STAMP(4);
      }
      if constexpr (n < 80) {
        const bf16x8 f = fragv(uf[n % RING]);
        ldf(n + AHEAD);
        sfor<8>([&](auto C) {
          constexpr int c = C;
          if constexpr (n < 12) {
            constexpr int e = 2 * c - n;
            if constexpr (e >= 3 && e <= 8) y[c] = mf16(f, H4[e - 3], y[c]);
          } else if constexpr (n < 16) {
            constexpr int e = c - (n - 12);
            if constexpr (e >= 4) y[c] = mf16(f, H8[e - 4], y[c]);
          } else {
            constexpr int m = 8 * c - (n - 23);
            if constexpr (m >= 0 && m <= 16) y[c] = mf16(Ff[m], f, y[c]);
            if constexpr (m == 32) y[c] = mf16(Ff[17], f, y[c]);
          }
        });
      }
      if constexpr (n >= 25 && (n - 25) % 8 == 0) {
        constexpr int c = (n - 25) / 8;
        *lp<u32x2>(sb + c * 512) = u32x2{pk2(y[c][0], y[c][1]), pk2(y[c][2], y[c][3])};
        CFENCE();
        if (c & 1) ov = *lp<u32x4>(yr + (c >> 1) * 1024);
      }
      if constexpr (n >= 39 && (n - 39) % 16 == 0) st16c(ov, rws, yo, ((n - 39) / 16) * 1024);
      SB();
    });
    STAMP(5);
    lds_set(SY_SDONE + (wave - 4) * 4, 1);
    if (wave == 4) lds_set(SY_SDONE + 12, 1);
    publish<8>(rws, WS_FC + d * 64, tag, lane);
    STAMP(6);
    if (wave == 4) {
      u32 pc_[2] = {(qn * 64 + lane) * 64, lane < 16 ? (512 + qn * 16 + lane) * 64 : OOR};
      lds_wait4(SY_SDONE, 1);
      __builtin_amdgcn_s_sleep(C_DELAY);
      poll_flags<2, C_SLEEP>(rws, pc_, WS_FC + g8 * REP, tag);
      STAMP(8);
      u32x4 sv[10];
#pragma unroll
      for (int i = 0; i < 10; i++) {
        const u32 d0 = i < 8 ? qn * 64 + i * 8 : 512 + qn * 16 + (i - 8) * 8;
        sv[i] = ld16c(rws, (lane >> 3) * 4096 + (lane & 7) * 16, WS_ST + d0 * 4096 + qm * 128);
      }
      lds_wait4(SY_M, 17);
      const u32 sb2 = opq(L_SIMG + (lane >> 3) * 128 + (((lane & 7) ^ (((lane >> 4) & 3) << 1)) << 4));
#pragma unroll
      for (int i = 0; i < 10; i++) *lp<u32x4>(sb2 + i * 1024) = sv[i];
      lds_wait4(SY_M, 18);
      lds_set(SY_EPI2, 1);
      STAMP(9);
    }
  } else {
    const rsrc_t rw2 = mk(w2, 1536 * 1280);
    const u32 io = (w >> 4) * 4 + (g8 >> 1);
    const u32 kr = min(io / 12, 4u), lg = (g8 & 1) * 12 + io % 12;
    __builtin_amdgcn_s_setprio(2);
    u32x4 e[3], v[16];
    lds_set(SY_SDONE + 8, 1);
    lds_set(SY_KD + 8, 1);
    __builtin_amdgcn_s_sleep(P_DELAY);
#pragma unroll
    for (int i = 0; i < 3; i++) {
      const u32 c = i * 64 + lane, q = c / 24, rr = c - q * 24;
      e[i] = ld16(rw2, (rr >> 1) * 1280 + q * 32 + (rr & 1) * 16, pair * 12 * 1280 + 1024);
    }
#pragma unroll
    for (int i = 0; i < 16; i++) v[i] = ld16(rw1, io < 60 ? (lane >> 3) * 3072 + (lane & 7) * 16 : OOR, (kr * 128 + i * 8) * 3072 + lg * 128);
    SB();
#define W2_TOUCH() asm volatile("s_mov_b32 m0, %0\n\ts_nop 0\n\tbuffer_load_dword %1, %3, %4 offen lds\n\tbuffer_load_dword %2, %3, %4 offen lds" \
                 ::"s"(L_DUMMY), "v"(vr + (lane & 7) * 128), "v"(lane < 32 ? vr + (lane & 7) * 128 + 8 * 1280 : OOR), "s"(rw2), "s"(pair * 12 * 1280))
#pragma unroll
    for (int i = 0; i < 3; i++) {
      const u32 c = i * 64 + lane, q = c / 24, rr = c - q * 24;
      st16c(e[i], rws, q * 49152 + rr * 16, WS_W2X + pair * 384);
    }
    SB();
    const u32x2 tag = get_tag();
    publish<1>(rws, WS_F1 + g8 * REP + (32 + (w >> 4)) * 64, tag, lane);
    STAMP(4);
    float s[8];
#pragma unroll
    for (int j = 0; j < 8; j++) s[j] = 0.f;
#pragma unroll
    for (int i = 0; i < 16; i++)
#pragma unroll
      for (int j = 0; j < 4; j++) { s[2 * j] += bflo(v[i][j]); s[2 * j + 1] += bfhi(v[i][j]); }
#pragma unroll
    for (int j = 0; j < 8; j++) s[j] = swap32_add(swap16_add(dpp_add<0x128>(s[j])));
    {
      const u32 pt = (lane >> 3) & 3;
      const float a = pt == 0 ? s[0] : pt == 1 ? s[2] : pt == 2 ? s[4] : s[6], b = pt == 0 ? s[1] : pt == 1 ? s[3] : pt == 2 ? s[5] : s[7];
      st16c(u32x4{__float_as_uint(a), tag.x, __float_as_uint(b), tag.y}, rws, (lane < 32 && io < 60) ? (lane & 7) * 64 + pt * 16 + WS_CSP + kr * 12288 + lg * 512 : OOR, 0);
    }
    STAMP(1);
    if (r5 == 31) {
      u32 po[1] = {lane < 48 ? lane * 64 : OOR};
      __builtin_amdgcn_s_sleep(G_DELAY);
      poll_flags<1, G_SLEEP>(rws, po, WS_F1 + g8 * REP, tag);
      publish<8>(rws, WS_FA + g8 * 128, tag, lane);
      STAMP(3);
    }
    VMCNT(0);
    STAMP(2);
  }
  STAMPS_OUT();
}
extern "C" __device__ __attribute__((used, noinline)) void s4mlp_end() {}

extern "C" void kernel_launch(void* const* d_in, const int* in_sizes, int n_in, void* d_out, int out_size, void* d_ws, size_t ws_size,
                              hipStream_t stream) {
  s4mlp<<<256, 512, 0, stream>>>((const u16*)d_in[0], (const u16*)d_in[1], (const u16*)d_in[2], (const u16*)d_in[3], (const u16*)d_in[4],
                                 (const u16*)d_in[5], (const u16*)d_in[6], (const u16*)d_in[7], (const u16*)d_in[8], (const u16*)d_in[10],
                                 (const u16*)d_in[11], (const u16*)d_in[12], (u16*)d_out, (char*)d_ws);
}
```

```cpp
#define C_AT 16
#define C_DELAY 10
#define W2_PF 4
#define NO_W1_PF
#define M_POLL
#include <hip/hip_runtime.h>
#include <stdint.h>
#include <utility>

typedef unsigned short u16;
typedef unsigned u32;
typedef unsigned long long u64;
typedef __attribute__((ext_vector_type(2))) float f32x2;
typedef __attribute__((ext_vector_type(4))) float f32x4;
typedef __attribute__((ext_vector_type(16))) float f32x16;
typedef __attribute__((ext_vector_type(4))) short s16x4;
typedef __attribute__((ext_vector_type(2))) __bf16 bf16x2;
typedef __attribute__((ext_vector_type(8))) __bf16 bf16x8;
typedef __attribute__((ext_vector_type(2))) _Float16 f16x2;
typedef __attribute__((ext_vector_type(8))) _Float16 f16x8;
typedef __attribute__((ext_vector_type(2))) u32 u32x2;
typedef __attribute__((ext_vector_type(4))) u32 u32x4;
typedef __amdgpu_buffer_rsrc_t rsrc_t;
#define DI __device__ __forceinline__
#define LAS __attribute__((address_space(3)))
#define SPIN_NAP()
#define CFENCE() asm volatile("" ::: "memory")
#define SB() __builtin_amdgcn_sched_barrier(0)

constexpr u32 REP = 65536 + 256;
constexpr u32 WS_EPOCH = 0, WS_REC = 1u << 19, WS_STG = 32768, WS_CSP = 65536, WS_FB = (14u << 20) + 524288, WS_FA = 15u << 20, WS_F1 = WS_FA + 4096, WS_FC = WS_FA + 24576,
              WS_W2X = 131072, WS_XT = 1u << 20, WS_ST = 4u << 20, WS_ACT = 8u << 20, WS_DBG = 16u << 20, WS_USED = 24u << 20;
constexpr u32 L_SSM = 0, SSM_SZ = 15488, L_XS = L_SSM + 3 * SSM_SZ, L_CS = L_XS + 10240, L_B1 = L_CS + 384, L_ST = L_B1 + 384,
              L_SY = L_ST + 1024, L_DUMMY = L_SY + 512, L_END = L_DUMMY + 512, RB = 61440;
constexpr u32 SY_M = L_SY, SY_TAG = L_SY + 16, SY_TAGOK = L_SY + 24, SY_EPI1 = L_SY + 80, SY_U = L_SY + 96, SY_FULL = L_SY + 36,
              SY_EPI2 = L_SY + 40, SY_W2X = L_SY + 44, SY_SDONE = L_SY + 48, SY_RED = L_SY + 64, SY_XF = L_SY + 112, SY_RS = L_SY + 224,
              SY_F0 = L_SY + 256, SY_KD = L_SY + 272, SY_B = L_SY + 288, SY_F = L_SY + 128;
constexpr u32 L_SIMG = RB, L_RED = RB + 16384 + 4096;
static_assert((L_RED & 16383) + 10240 <= 16384 && L_RED + 16384 + 10240 <= RB + 49152, "red");
static_assert(L_END <= RB && RB + 102400 <= 163840, "lds");
constexpr u32 SPIN_CAP = 1u << 17, LSPIN_CAP = 1u << 22;
#define W2_PF_AT 7
#define PK_ROUNDS 1
#define POLL_AT 7
constexpr float LOG2E = 1.4426950408889634f, LN2 = 0.6931471805599453f;

constexpr u32 T4 = 5376, T8 = 5888, S5 = 16, S3 = 3216, S9 = 6400, S7 = 8752, LB_DUM = 7168;
__shared__ __attribute__((aligned(1024))) char smem[163840];

template <class F, int... I> DI void sfor_(F&& f, std::integer_sequence<int, I...>) { (f(std::integral_constant<int, I>{}), ...); }
template <int N, class F> DI void sfor(F&& f) { sfor_(f, std::make_integer_sequence<int, N>{}); }
template <class T> DI LAS T* lp(u32 off) { return (LAS T*)((LAS char*)smem + off); }
DI u32 opq(u32 x) { asm volatile("" : "+v"(x)); return x; }
DI u32 rfl(u32 x) { return __builtin_amdgcn_readfirstlane(x); }
DI float bflo(u32 v) { return __uint_as_float(v << 16); }
DI float bfhi(u32 v) { return __uint_as_float(v & 0xffff0000u); }
DI u32 pk2(float a, float b) { bf16x2 v = {(__bf16)a, (__bf16)b}; return __builtin_bit_cast(u32, v); }
DI float rbf(float a) { return (float)(__bf16)a; }
DI rsrc_t mk(const void* p, u32 n) { return __builtin_amdgcn_make_buffer_rsrc((void*)p, (short)0, (int)n, 0x00020000); }
DI u32x4 ld16c(rsrc_t r, u32 vo, u32 so) { return __builtin_amdgcn_raw_buffer_load_b128(r, vo, so, 16); }
DI u32x2 ld8c(rsrc_t r, u32 vo, u32 so) { return __builtin_amdgcn_raw_buffer_load_b64(r, vo, so, 16); }
DI u32 ld4c(rsrc_t r, u32 vo, u32 so) { return __builtin_amdgcn_raw_buffer_load_b32(r, vo, so, 16); }
DI void st16c(u32x4 v, rsrc_t r, u32 vo, u32 so) { __builtin_amdgcn_raw_buffer_store_b128(v, r, vo + so, 0, 16); }
DI void st16(u32x4 v, rsrc_t r, u32 vo, u32 so) { __builtin_amdgcn_raw_buffer_store_b128(v, r, vo + so, 0, 16); }
DI void st8c(u32x2 v, rsrc_t r, u32 vo, u32 so) { __builtin_amdgcn_raw_buffer_store_b64(v, r, vo, so, 16); }
DI void st4c(u32 v, rsrc_t r, u32 vo, u32 so) { __builtin_amdgcn_raw_buffer_store_b32(v, r, vo, so, 16); }
DI u32x4 ld16(rsrc_t r, u32 vo, u32 so) { return __builtin_amdgcn_raw_buffer_load_b128(r, vo, so, 0); }
#define VMCNT(n) asm volatile("s_waitcnt vmcnt(" #n ")" ::: "memory")

DI void dma(u32 lds, u32 vo, rsrc_t r, u32 so) {
  asm volatile("s_mov_b32 m0, %0\n\ts_nop 0\n\tbuffer_load_dwordx4 %1, %2, %3 offen lds" ::"s"(lds), "v"(vo), "s"(r), "s"(so));
}
template <int OFF> DI void dma_next(u32 vo, rsrc_t r, u32 so) {
  asm volatile("buffer_load_dwordx4 %0, %1, %2 offen offset:%3 lds" ::"v"(vo), "s"(r), "s"(so), "i"(OFF));
}
DI void dma_flag(u32 lds, rsrc_t r) {
  asm volatile("s_mov_b64 exec, 1\n\ts_mov_b32 m0, %0\n\ts_nop 0\n\tbuffer_load_dword off, %1, 0 lds\n\ts_mov_b64 exec, -1" ::"s"(lds), "s"(r) : "memory");
}
DI void dma3(u32 lds, u32 v0, u32 v1, u32 v2, rsrc_t r, u32 so) {
  asm volatile("s_mov_b32 m0, %0\n\ts_nop 0\n\tbuffer_load_dwordx4 %1, %4, %5 offen lds\n\tbuffer_load_dwordx4 %2, %4, %5 offen offset:1024 lds\n\t"
               "buffer_load_dwordx4 %3, %4, %5 offen offset:2048 lds" ::"s"(lds), "v"(v0), "v"(v1), "v"(v2), "s"(r), "s"(so));
}
DI void dma4(u32 lds, u32 v0, u32 v1, u32 v2, u32 v3, rsrc_t r, u32 so) {
  asm volatile("s_mov_b32 m0, %0\n\ts_nop 0\n\tbuffer_load_dwordx4 %1, %5, %6 offen lds\n\tbuffer_load_dwordx4 %2, %5, %6 offen offset:1024 lds\n\t"
               "buffer_load_dwordx4 %3, %5, %6 offen offset:2048 lds\n\tbuffer_load_dwordx4 %4, %5, %6 offen offset:3072 lds"
               ::"s"(lds), "v"(v0), "v"(v1), "v"(v2), "v"(v3), "s"(r), "s"(so));
}
DI void dmac(u32 lds, u32 vo, rsrc_t r, u32 so) {
  asm volatile("s_mov_b32 m0, %0\n\ts_nop 0\n\tbuffer_load_dwordx4 %1, %2, %3 offen sc1 lds" ::"s"(lds), "v"(vo), "s"(r), "s"(so));
}
DI void dma2c(u32 lds, u32 vo, rsrc_t r, u32 so) {
  asm volatile("s_mov_b32 m0, %0\n\ts_nop 0\n\tbuffer_load_dwordx4 %1, %2, %3 offen sc1 lds\n\tbuffer_load_dwordx4 %1, %2, %3 offen offset:1024 sc1 lds"
               ::"s"(lds), "v"(vo), "s"(r), "s"(so));
}
DI u32x2 trd(u32 off) { return __builtin_bit_cast(u32x2, __builtin_amdgcn_ds_read_tr16_b64_v4i16(lp<s16x4>(off))); }
DI bf16x8 frag(u32x2 a, u32x2 b) { u32x4 v = {a.x, a.y, b.x, b.y}; return __builtin_bit_cast(bf16x8, v); }
DI bf16x8 fragv(u32x4 v) { return __builtin_bit_cast(bf16x8, v); }
DI f32x4 mf16(bf16x8 a, bf16x8 b, f32x4 c) { return __builtin_amdgcn_mfma_f32_16x16x32_bf16(a, b, c, 0, 0, 0); }
DI f32x16 mf32(bf16x8 a, bf16x8 b, f32x16 c) { return __builtin_amdgcn_mfma_f32_32x32x16_bf16(a, b, c, 0, 0, 0); }

DI void lds_set(u32 off, u32 v) { CFENCE(); *(volatile LAS u32*)lp<u32>(off) = v; CFENCE(); }
DI void lds_wait(u32 off, u32 v) {
  CFENCE();
#pragma nounroll
  for (u32 n = 0; rfl(*(volatile LAS u32*)lp<u32>(off)) < v && n < LSPIN_CAP; n++) __builtin_amdgcn_s_sleep(1);
  CFENCE();
}
DI u32 lds_min4(u32 off) {
  CFENCE();
  u32x4 x = *(volatile LAS u32x4*)lp<u32x4>(off);
  u32 m = rfl(min(min(x.x, x.y), min(x.z, x.w)));
  CFENCE();
  return m;
}
DI void lds_wait4(u32 off, u32 v) {
#pragma nounroll
  for (u32 n = 0; lds_min4(off) < v && n < LSPIN_CAP; n++) __builtin_amdgcn_s_sleep(1);
}
DI void lds_spin4(u32 off, u32 v) {
#pragma nounroll
  for (u32 n = 0; lds_min4(off) < v && n < LSPIN_CAP; n++) { SPIN_NAP(); }
}
DI u32x2 get_tag() { lds_wait(SY_TAGOK, 1); return *lp<u32x2>(SY_TAG); }
#define A_SLEEP 4
#define A_DELAY 16
#define PARK_AT 12
#define G2_WIN 7
#define XT_PACE 2
#define ST_PACE 0
#ifndef M_PRIO
#define M_PRIO 0
#endif
#define S_PRIO 1
#define L_DELAY 4
#define P_DELAY 1
#define BULK_DELAY 5
#define VMX "29"
#define YIELD_AT 8
#define G_DELAY 1
#define G_SLEEP 2
#define PNT ""
#define FORCE_SLOW false
#define B_DELAY 30
#define B_SLEEP 10
#define C_SLEEP 12
constexpr u32 OOR = 0x7ffffff0u;
template <int NL, int SLEEP> DI void poll_flags(rsrc_t ws, u32 (&off)[NL], u32 so, u32x2 tag) {
  u64 pend[NL];
#pragma unroll
  for (int i = 0; i < NL; i++) pend[i] = 1;
#pragma nounroll
  for (u32 spin = 0; spin < SPIN_CAP; spin++) {
    CFENCE();
    u32x2 v[NL];
#pragma unroll
    for (int i = 0; i < NL; i++) { v[i] = tag; if (NL == 1 || pend[i]) v[i] = ld8c(ws, off[i], so); }
    u64 any = 0;
#pragma unroll
    for (int i = 0; i < NL; i++) {
      if (v[i].x == tag.x && v[i].y == tag.y) off[i] = OOR;
      pend[i] = __builtin_amdgcn_ballot_w64(off[i] != OOR);
      any |= pend[i];
    }
    if (any == 0) break;
    __builtin_amdgcn_s_sleep(SLEEP);
  }
  CFENCE();
}
template <int NL, int SLEEP> DI void poll_packed(rsrc_t ws, u32 vo, u32 so, u32x2 tag) {
  u32 off[NL];
#pragma unroll
  for (int i = 0; i < NL; i++) off[i] = vo + i * 1024;
#pragma nounroll
  for (u32 spin = 0; spin < SPIN_CAP; spin++) {
    CFENCE();
    u32x4 v[NL];
#pragma unroll
    for (int i = 0; i < NL; i++) v[i] = ld16c(ws, off[i], so);
    bool pend = false;
#pragma unroll
    for (int i = 0; i < NL; i++) {
      if (v[i].x == tag.x && v[i].y == tag.y && v[i].z == tag.x && v[i].w == tag.y) off[i] = OOR;
      pend = pend || off[i] != OOR;
    }
    if (__builtin_amdgcn_ballot_w64(pend) == 0) break;
    __builtin_amdgcn_s_sleep(SLEEP);
  }
  CFENCE();
}
template <int R> DI void publish(rsrc_t ws, u32 so, u32x2 tag, int lane) {
  VMCNT(0);
  st8c(tag, ws, lane < R ? lane * REP : OOR, so);
  CFENCE();
}

#define STAMP(i)
#define STAMPS_DECL
#define STAMPS_GO
#define STAMPS_OUT()

template <bool PK> DI f32x2 cmul(f32x2 a, f32x2 b) {
  if constexpr (PK) {
    f32x2 bs = {-b.y, b.x};
    f32x2 r = a.xx * b;
    return __builtin_elementwise_fma(a.yy, bs, r);
  } else {
    float t; asm("v_mul_f32 %0, %1, %2" : "=v"(t) : "v"(a.x), "v"(b.x));
    return f32x2{__builtin_fmaf(-a.y, b.y, t), __builtin_fmaf(a.y, b.x, a.x * b.y)};
  }
}
DI f32x2 csq(f32x2 a) {
  float t; asm("v_mul_f32 %0, %1, %1" : "=v"(t) : "v"(a.x));
  return f32x2{__builtin_fmaf(-a.y, a.y, t), (a.x + a.x) * a.y};
}
DI u32 toh(f32x2 v) { u32 r; asm("v_cvt_pk_f16_f32 %0, %1, %2" : "=v"(r) : "v"(v.x), "v"(v.y)); return r; }
template <bool CONJ> DI void hmul2(u32& r0, u32 a0, u32 b0, u32& r1, u32 a1, u32 b1) {
  if constexpr (!CONJ)
    asm("v_pk_mul_f16 %0, %2, %3 op_sel_hi:[0,1]\n\tv_pk_mul_f16 %1, %4, %5 op_sel_hi:[0,1]\n\t"
        "v_pk_fma_f16 %0, %2, %3, %0 op_sel:[1,1,0] op_sel_hi:[1,0,1] neg_lo:[0,1,0]\n\tv_pk_fma_f16 %1, %4, %5, %1 op_sel:[1,1,0] op_sel_hi:[1,0,1] neg_lo:[0,1,0]"
        : "=&v"(r0), "=&v"(r1) : "v"(a0), "v"(b0), "v"(a1), "v"(b1));
  else
    asm("v_pk_mul_f16 %0, %2, %3 op_sel_hi:[0,1] neg_hi:[0,1]\n\tv_pk_mul_f16 %1, %4, %5 op_sel_hi:[0,1] neg_hi:[0,1]\n\t"
        "v_pk_fma_f16 %0, %2, %3, %0 op_sel:[1,1,0] op_sel_hi:[1,0,1]\n\tv_pk_fma_f16 %1, %4, %5, %1 op_sel:[1,1,0] op_sel_hi:[1,0,1]"
        : "=&v"(r0), "=&v"(r1) : "v"(a0), "v"(b0), "v"(a1), "v"(b1));
}
DI f32x4 mfh(u32x4 a, u32x4 b, f32x4 c) { return __builtin_amdgcn_mfma_f32_16x16x32_f16(__builtin_bit_cast(f16x8, a), __builtin_bit_cast(f16x8, b), c, 0, 0, 0); }
DI f32x2 cexp2(float lr, float th, float sgn) {
  float m = __builtin_amdgcn_exp2f(lr), f = __builtin_amdgcn_fractf(th);
  f32x2 r = {m * __builtin_amdgcn_cosf(f), sgn * m * __builtin_amdgcn_sinf(f)};
  return r;
}
template <int CTRL> DI float dpp_add(float v) {
  return v + __builtin_bit_cast(float, __builtin_amdgcn_update_dpp(0, __builtin_bit_cast(int, v), CTRL, 0xf, 0xf, true));
}
template <int CTRL> DI float dpp_max(float v) {
  return fmaxf(v, __builtin_bit_cast(float, __builtin_amdgcn_update_dpp(0, __builtin_bit_cast(int, v), CTRL, 0xf, 0xf, true)));
}
DI float wave_max(float v) {
  v = dpp_max<0x128>(v); v = dpp_max<0x124>(v); v = dpp_max<0x122>(v); v = dpp_max<0x121>(v);
  const float a = __builtin_bit_cast(float, __builtin_amdgcn_readlane(__builtin_bit_cast(int, v), 0)), b = __builtin_bit_cast(float, __builtin_amdgcn_readlane(__builtin_bit_cast(int, v), 16)),
              c = __builtin_bit_cast(float, __builtin_amdgcn_readlane(__builtin_bit_cast(int, v), 32)), d = __builtin_bit_cast(float, __builtin_amdgcn_readlane(__builtin_bit_cast(int, v), 48));
  return fmaxf(fmaxf(a, b), fmaxf(c, d));
}
DI float swap16_add(float v) { auto r = __builtin_amdgcn_permlane16_swap(__float_as_uint(v), __float_as_uint(v), false, false); return __uint_as_float(r[0]) + __uint_as_float(r[1]); }
DI float swap32_add(float v) { auto r = __builtin_amdgcn_permlane32_swap(__float_as_uint(v), __float_as_uint(v), false, false); return __uint_as_float(r[0]) + __uint_as_float(r[1]); }
DI float red32(float v) {
  v = dpp_add<0x128>(v); v = dpp_add<0x124>(v); v = dpp_add<0x122>(v); v = dpp_add<0x121>(v);
  return swap16_add(v);
}
DI u32x4 ldflags4(u32 off) { CFENCE(); u32x4 x = *(volatile LAS u32x4*)lp<u32x4>(off); CFENCE(); return x; }
DI u32 ldflag(u32 off) { CFENCE(); u32 x = *(volatile LAS u32*)lp<u32>(off); CFENCE(); return x; }
DI void chk4(u32x4 x, u32 off, u32 v) {
  if (rfl(min(min(x.x, x.y), min(x.z, x.w))) < v) {
#pragma nounroll
    for (u32 n = 0; lds_min4(off) < v && n < LSPIN_CAP; n++) { SPIN_NAP(); }
  }
  CFENCE();
}
DI void chk1(u32 x, u32 off, u32 v) {
  if (rfl(x) < v) {
#pragma nounroll
    for (u32 n = 0; rfl(ldflag(off)) < v && n < LSPIN_CAP; n++) { SPIN_NAP(); }
  }
  CFENCE();
}

template <int NR> DI void kgen(const u32 (&pw)[6], u32 ldt, u32 LB, u32 lane, f32x4 (&ka)[2][2]) {
  const u32 c16 = lane & 15, gam = lane >> 4;
  const u32 sw = opq(LB + lane * 4), sr = opq(LB + c16 * 288 + gam * 16), sr2 = opq(LB + (16 + min(c16, 4u)) * 288 + gam * 16);
  const u32 sqa = opq(LB + (21 + (c16 & 3)) * 288 + gam * 16), sqb = opq(LB + (c16 < 4 ? 21 : 24 + (c16 >> 2)) * 288 + gam * 16), sqc = opq(LB + 28 * 288 + gam * 16);
  *lp<u32>(sw + 21 * 288) = 0x3c00u;
  SB();
  const float dt = rbf(__builtin_amdgcn_exp2f(bflo(ldt) * LOG2E));
#pragma unroll
  for (int i = 0; i < 4; i++) ka[i >> 1][i & 1] = f32x4{0, 0, 0, 0};
  u32x4 qa[4], qb_[4], qc[4], pf[2][4];
  auto kq = [&]() {
    u32x4 q0[4], q1[4];
#pragma unroll
    for (int k = 0; k <= 4; k++) {
      u32 a0, a1, a2, a3, b0, b1_, b2, b3;
      if (k < 4) hmul2<false>(a0, qa[k].x, qb_[k].x, a1, qa[k].y, qb_[k].y);
      else asm volatile("s_nop 1" : "+v"(q1[3]));
      SB();
      if (k > 0) ka[0][0] = mfh(pf[0][k - 1], q0[k - 1], ka[0][0]);
      SB();
      if (k < 4) hmul2<false>(a2, qa[k].z, qb_[k].z, a3, qa[k].w, qb_[k].w);
      SB();
      if (k > 0) ka[1][0] = mfh(pf[1][k - 1], q0[k - 1], ka[1][0]);
      SB();
      if (k < 4) hmul2<false>(b0, a0, qc[k].x, b1_, a1, qc[k].y);
      SB();
      if (k > 0) ka[0][1] = mfh(pf[0][k - 1], q1[k - 1], ka[0][1]);
      SB();
      if (k < 4) hmul2<false>(b2, a2, qc[k].z, b3, a3, qc[k].w);
      SB();
      if (k > 0) ka[1][1] = mfh(pf[1][k - 1], q1[k - 1], ka[1][1]);
      SB();
      if (k < 4) { q0[k] = u32x4{a0, a1, a2, a3}; q1[k] = u32x4{b0, b1_, b2, b3}; }
    }
  };
  auto prm = [&](int r, int i) { return r == 0 ? bflo(pw[i]) : bfhi(pw[i]); };
  {
    bool grow = false;
#pragma unroll
    for (int r = 0; r < NR; r++) grow = grow || FORCE_SLOW || dt * prm(r, 0) > 0.f;
    if (__builtin_amdgcn_ballot_w64(grow) != 0) {
#pragma nounroll
      for (int r = 0; r < NR; r++) {
        const u32 sh = 16 - 16 * r;
        auto pr = [&](int i) { return __uint_as_float((pw[i] << sh) & 0xffff0000u); };
        const float lr = dt * pr(0) * LOG2E, th = dt * pr(1) * 0.15915494309189535f;
        const f32x2 wc = cmul<false>(f32x2{pr(2), -pr(3)}, f32x2{pr(4), -pr(5)}) * (dt * LN2);
#pragma nounroll
        for (int j = 0; j < 32; j++) { const f32x2 q = cexp2(lr * (8 * j), th * (8 * j), 1.f); *lp<u32>(sw + j * 288) = pk2(q.x, q.y); }
        CFENCE();
#pragma unroll
        for (int k = 0; k < 4; k++) { qa[k] = *lp<u32x4>(sr + k * 64); qb_[k] = *lp<u32x4>(sr + 16 * 288 + k * 64); }
        CFENCE();
#pragma nounroll
        for (int i = 0; i < 21; i++) {
          const float e = i < 8 ? i : i < 12 ? 240 + 2 * i : i < 16 ? 512 + 256 * ((i - 12) >> 1) + 4 * (i & 1) : 256 * i - 3072;
          const f32x2 p = cmul<false>(wc, cexp2(lr * e, th * e, -1.f));
          *lp<u32>(sw + i * 288) = pk2(p.x, p.y);
        }
        CFENCE();
#pragma unroll
        for (int k = 0; k < 4; k++) {
          const bf16x8 p0 = fragv(*lp<u32x4>(sr + k * 64)), p1 = fragv(*lp<u32x4>(sr2 + k * 64));
          ka[0][0] = mf16(p0, fragv(qa[k]), ka[0][0]); ka[0][1] = mf16(p0, fragv(qb_[k]), ka[0][1]);
          ka[1][0] = mf16(p1, fragv(qa[k]), ka[1][0]); ka[1][1] = mf16(p1, fragv(qb_[k]), ka[1][1]);
        }
        CFENCE();
      }
      return;
    }
  }
  u32 wh[NR];
  float unscale;
  {
    f32x2 wv[NR];
    float mx = 0.f;
#pragma unroll
    for (int r = 0; r < NR; r++) {
      wv[r] = cmul<false>(f32x2{prm(r, 2), -prm(r, 3)}, f32x2{prm(r, 4), -prm(r, 5)});
      mx = fmaxf(mx, fmaxf(fabsf(wv[r].x), fabsf(wv[r].y)));
    }
    const int ex = __builtin_amdgcn_frexp_expf(wave_max(mx));
#pragma unroll
    for (int r = 0; r < NR; r++) wh[r] = toh(f32x2{__builtin_amdgcn_ldexpf(wv[r].x, 14 - ex), __builtin_amdgcn_ldexpf(wv[r].y, 14 - ex)});
    unscale = __builtin_amdgcn_ldexpf(dt * LN2, ex - 14);
  }
  sfor<NR>([&](auto R) {
    constexpr int r = R;
    const float lr = dt * prm(r, 0) * LOG2E, th = dt * prm(r, 1) * 0.15915494309189535f;
    u32 zh[11];
    {
      f32x2 za = cexp2(lr, th, 1.f), zb = cexp2(lr * 32.f, th * 32.f, 1.f);
      zh[0] = toh(za); zh[5] = toh(zb);
#pragma unroll
      for (int i = 1; i < 6; i++) { if (i < 5) { za = csq(za); zh[i] = toh(za); } zb = csq(zb); zh[5 + i] = toh(zb); }
    }
    u32 p[21], z24, z96;
    p[0] = wh[r];
    hmul2<false>(z24, zh[3], zh[4], z96, zh[5], zh[6]);
    *lp<u32>(sw + 22 * 288) = zh[3]; *lp<u32>(sw + 23 * 288) = zh[4]; *lp<u32>(sw + 24 * 288) = z24; *lp<u32>(sw + 25 * 288) = zh[5];
    *lp<u32>(sw + 26 * 288) = zh[6]; *lp<u32>(sw + 27 * 288) = z96; *lp<u32>(sw + 28 * 288) = zh[7];
    hmul2<true>(p[1], p[0], zh[0], p[2], p[0], zh[1]);
    hmul2<true>(p[4], p[0], zh[2], p[8], p[0], zh[8]);
    hmul2<true>(p[12], p[0], zh[9], p[16], p[0], zh[10]);
    hmul2<true>(p[3], p[1], zh[1], p[5], p[1], zh[2]);
    hmul2<true>(p[6], p[2], zh[2], p[9], p[2], zh[8]);
    hmul2<true>(p[10], p[4], zh[8], p[13], p[4], zh[9]);
    hmul2<true>(p[14], p[8], zh[9], p[17], p[8], zh[10]);
    hmul2<true>(p[18], p[12], zh[10], p[20], p[16], zh[10]);
    hmul2<true>(p[7], p[3], zh[2], p[11], p[6], zh[8]);
    hmul2<true>(p[15], p[10], zh[9], p[19], p[14], zh[10]);
    SB();
#pragma unroll
    for (int i = 0; i < 21; i++) *lp<u32>(sw + i * 288) = p[i];
    CFENCE();
    if (r > 0) { SB(); kq(); }
    SB();
#pragma unroll
    for (int k = 0; k < 4; k++) { qa[k] = *lp<u32x4>(sqa + k * 64); qb_[k] = *lp<u32x4>(sqb + k * 64); qc[k] = *lp<u32x4>(sqc + k * 64); pf[0][k] = *lp<u32x4>(sr + k * 64); pf[1][k] = *lp<u32x4>(sr2 + k * 64); }
    CFENCE();
    SB();
  });
  kq();
#pragma unroll
  for (int i = 0; i < 4; i++) ka[i >> 1][i & 1] *= unscale;
}

extern "C" __global__ void __launch_bounds__(512, 2)
s4mlp(const u16* __restrict__ x, const u16* __restrict__ Are, const u16* __restrict__ Aim, const u16* __restrict__ Bre,
      const u16* __restrict__ Bim, const u16* __restrict__ Cre, const u16* __restrict__ Cim, const u16* __restrict__ logdt,
      const u16* __restrict__ Dsk, const u16* __restrict__ w1, const u16* __restrict__ b1, const u16* __restrict__ w2,
      u16* __restrict__ out, char* __restrict__ wsp) {
  const int lane = threadIdx.x & 63;
  const u32 wave = rfl(threadIdx.x >> 6);
  const u32 w = blockIdx.x, g8 = w & 7, r5 = w >> 3;
  STAMPS_DECL;
  rsrc_t rc;
  {
    u64 pc; u32 back, len;
    asm volatile("s_getpc_b64 %0\n1:\n\ts_mov_b32 %1, 1b-s4mlp\n\ts_mov_b32 %2, s4mlp_end-s4mlp" : "=s"(pc), "=s"(back), "=s"(len));
    rc = mk((const char*)(pc - back), len);
  }
  asm volatile("s_cmp_lg_u32 %3, 0\n\ts_cbranch_scc1 2f\n\ts_mov_b32 m0, %0\n\ts_nop 0\n\tbuffer_load_dword %1, %2, 0 offen lds\n2:"
               ::"s"(L_DUMMY), "v"(((threadIdx.x << 3) + (r5 & 7)) << 7), "s"(rc), "s"(wave) : "scc");
  *lp<u32x2>(L_SY + lane * 8) = u32x2{0, 0};
  const u32 pm = (g8 >> 1) * 4 + (r5 >> 3), pn = (g8 & 1) * 8 + (r5 & 7);
  const u32 qm = g8 * 4 + (r5 >> 3), qn = r5 & 7;
  const u32 pair = (w >> 4) * 8 + g8, odd = r5 & 1;
  const u32 ch0 = pair * 5 + odd * 3, nch = 3 - odd;
  const u32 rot = ((r5 >> 3) ^ r5) & 1;
  constexpr u32 rot1 = 0;
  auto ck = [&](u32 j) { return j < 6 ? j + 6 * rot : j - 6 * rot; };
  const rsrc_t rws = mk(wsp, WS_USED);
  const rsrc_t rx = mk(x, 2048 * 1280);
  const u32 c16 = lane & 15, gam = lane >> 4, tq = (lane >> 2) & 3, tp = lane & 3;

  const u32 tm = pm * 2 + (pn >> 3), tn = pn & 7;
  u32 vx[4];
#pragma unroll
  for (int j = 0; j < 4; j++) vx[j] = (lane >> 3) * 1280 + (((lane & 7) ^ (4 * (j & 1) + (lane >> 4))) << 4) + j * 9216;
  const u32 ldx = RB + wave * 4096;
  const u32 soxr[2] = {(pm * 128 + wave * 32) * 1280 + rot1 * 640, (pm * 128 + wave * 32) * 1280 - rot1 * 640};
  auto pieceX = [&](int i, int s) {
    if (i == 0) dma(ldx + ((s + 2) % 3) * 16384, vx[0], rx, soxr[s >= 5] + s * 128);
    if (i == 1) dma_next<1024>(vx[1], rx, soxr[s >= 5] + s * 128);
    if (i == 2) dma_next<2048>(vx[2], rx, soxr[s >= 5] + s * 128);
    if (i == 3) dma_next<3072>(vx[3], rx, soxr[s >= 5] + s * 128);
  };
  const rsrc_t rw1 = mk(w1, 640 * 3072);
  u32 vw[3];
#pragma unroll
  for (int j = 0; j < 3; j++) vw[j] = (((64 * j + lane) * 171) >> 11) * 2880 + lane * 16;
  const u32 d = min(ch0 + (wave & 3), 639u);
  u32 pw[6], ldt2 = 0, dsk2 = 0;
  asm volatile("" : "+v"(vx[0]), "+v"(vx[1]), "+v"(vx[2]), "+v"(vx[3]), "+v"(vw[0]), "+v"(vw[1]), "+v"(vw[2]));
  asm volatile("s_waitcnt lgkmcnt(0)\n\ts_barrier" ::"s"(x), "s"(Are), "s"(Aim), "s"(Bre), "s"(Bim), "s"(Cre), "s"(Cim), "s"(logdt), "s"(Dsk), "s"(w1), "s"(b1), "s"(w2), "s"(out), "s"(wsp) : "memory");
  STAMPS_GO;
  STAMP(0);
  if ((wave & 3) < nch) {
    const u32 ob = d * 384, o = wave < 4 ? 256 + lane * 2 : lane * 4, od = (d >> 1) * 4;
    const char *p0 = (const char*)Are + ob, *p1 = (const char*)Aim + ob, *p2 = (const char*)Bre + ob, *p3 = (const char*)Bim + ob, *p4 = (const char*)Cre + ob, *p5 = (const char*)Cim + ob;
    asm volatile("global_load_dword %0, %2, %3\n\tglobal_load_dword %1, %2, %4" : "=&v"(ldt2), "=&v"(dsk2) : "v"(od), "s"(logdt), "s"(Dsk));
    if (wave < 4)
      asm volatile("global_load_ushort %2, %6, %9" PNT "\n\tglobal_load_ushort %3, %6, %10" PNT "\n\tglobal_load_ushort %4, %6, %11" PNT "\n\tglobal_load_ushort %5, %6, %12" PNT "\n\tglobal_load_ushort %0, %6, %7" PNT "\n\tglobal_load_ushort %1, %6, %8" PNT
                   : "=&v"(pw[0]), "=&v"(pw[1]), "=&v"(pw[2]), "=&v"(pw[3]), "=&v"(pw[4]), "=&v"(pw[5]) : "v"(o), "s"(p0), "s"(p1), "s"(p2), "s"(p3), "s"(p4), "s"(p5));
    else
      asm volatile("global_load_dword %2, %6, %9" PNT "\n\tglobal_load_dword %3, %6, %10" PNT "\n\tglobal_load_dword %4, %6, %11" PNT "\n\tglobal_load_dword %5, %6, %12" PNT "\n\tglobal_load_dword %0, %6, %7" PNT "\n\tglobal_load_dword %1, %6, %8" PNT
                   : "=&v"(pw[0]), "=&v"(pw[1]), "=&v"(pw[2]), "=&v"(pw[3]), "=&v"(pw[4]), "=&v"(pw[5]) : "v"(o), "s"(p0), "s"(p1), "s"(p2), "s"(p3), "s"(p4), "s"(p5));
  }
  __builtin_amdgcn_s_sleep(BULK_DELAY);
  if (wave < 4) {
    dma3(RB + 49152 + 2 * 12288 + wave * 3072, vw[0], vw[1], vw[2], rw1, pn * 192 + 5 * rot1 * (64 * 3072) + wave * 16 * 3072);
#pragma unroll
    for (int i = 0; i < 4; i++) pieceX(i, 0);
    dma_flag(SY_F0 + wave * 4, rc);
  }
  asm volatile("s_cmp_lt_u32 %8, 4\n\ts_cbranch_scc1 3f\n\ts_waitcnt vmcnt(0)\n3:\n\ts_waitcnt vmcnt(8)"
               : "+v"(pw[0]), "+v"(pw[1]), "+v"(pw[2]), "+v"(pw[3]), "+v"(pw[4]), "+v"(pw[5]), "+v"(ldt2), "+v"(dsk2) : "s"(wave) : "memory", "scc");
  const u32 ldt = ldt2 >> ((d & 1) * 16), dsk = dsk2 >> ((d & 1) * 16);
  asm volatile("" ::: "memory");
  const u32 wave2 = (u32)__builtin_amdgcn_readfirstlane(opq(wave));
  const u32 vr = (lane >> 3) * 1280, ve = (lane >> 1) * 1280 + (lane & 1) * 16, vl = lane * 16;
  auto tile = [&](u32 v0, u32 v1, u32 r64, u32 cn) {
    const u32 so = r64 * 64 * 1280;
    dma4(L_XS, v0, v1 + 9216, v0 + 2 * 9216, v1 + 3 * 9216, rx, so + cn * 128);
    dma4(L_XS + 4096, v0, v1 + 9216, v0 + 2 * 9216, v1 + 3 * 9216, rx, so + cn * 128 + 32 * 1280);
    dma(L_XS + 8192, ve, rx, so + 1024 + cn * 32);
    dma(L_XS + 9216, ve, rx, so + 1024 + cn * 32 + 32 * 1280);
  };
  if (wave2 < 4) {
    __builtin_amdgcn_s_setprio(2);
    const u32 sym = SY_M + wave * 4;
    if (wave == 3) {
      u32x4 xr[10];
#pragma unroll
      for (int i = 0; i < 10; i++) asm volatile("buffer_load_dwordx4 %0, %1, %2, %3 offen offset:%4" : "=v"(xr[i]) : "v"(vr + (lane & 7) * 16), "s"(rx), "s"((pm * 128 + (pn ^ 8) * 8) * 1280), "i"(i * 128));
#pragma unroll
      for (int i = 0; i < 4; i++) {
        dma3(RB + 49152 + 3 * 12288 + i * 3072, vw[0], vw[1], vw[2], rw1, pn * 192 + (1 + 5 * rot1) * (64 * 3072) + i * 16 * 3072);
        dma4(RB + i * 4096, vx[0], vx[1], vx[2], vx[3], rx, (pm * 128 + i * 32) * 1280 + rot1 * 640 + 128);
      }
      dma_flag(SY_F + 4, rc);
      if (!odd) {
        const rsrc_t rw2 = mk(w2, 1536 * 1280);
        asm volatile("s_mov_b32 m0, %0\n\ts_nop 0\n\tbuffer_load_dword %1, %3, %4 offen lds\n\tbuffer_load_dword %2, %3, %4 offen lds"
                     ::"s"(L_DUMMY), "v"(vr + (lane & 7) * 128), "v"(lane < 32 ? vr + (lane & 7) * 128 + 8 * 1280 : OOR), "s"(rw2), "s"(pair * 12 * 1280));
      }
      asm volatile("s_waitcnt vmcnt(" VMX ")" : "+v"(xr[0]), "+v"(xr[1]), "+v"(xr[2]), "+v"(xr[3]), "+v"(xr[4]), "+v"(xr[5]), "+v"(xr[6]), "+v"(xr[7]), "+v"(xr[8]), "+v"(xr[9])::"memory");
      float s0 = 0.f, s1 = 0.f, q0 = 0.f, q1 = 0.f;
#pragma unroll
      for (int i = 0; i < 10; i++)
        asm("v_dot2_f32_bf16 %0, %4, %8, %0\n\tv_dot2_f32_bf16 %2, %4, %4, %2\n\tv_dot2_f32_bf16 %1, %5, %8, %1\n\tv_dot2_f32_bf16 %3, %5, %5, %3\n\t"
            "v_dot2_f32_bf16 %0, %6, %8, %0\n\tv_dot2_f32_bf16 %2, %6, %6, %2\n\tv_dot2_f32_bf16 %1, %7, %8, %1\n\tv_dot2_f32_bf16 %3, %7, %7, %3"
            : "+v"(s0), "+v"(s1), "+v"(q0), "+v"(q1) : "v"(xr[i].x), "v"(xr[i].y), "v"(xr[i].z), "v"(xr[i].w), "v"(0x3f803f80u));
      asm volatile("s_nop 3" : "+v"(s0), "+v"(s1), "+v"(q0), "+v"(q1));
      float sm = s0 + s1, sq = q0 + q1;
      sm = dpp_add<0xb1>(sm); sq = dpp_add<0xb1>(sq); sm = dpp_add<0x4e>(sm); sq = dpp_add<0x4e>(sq); sm = dpp_add<0x141>(sm); sq = dpp_add<0x141>(sq);
      const float mean = sm * (1.f / 640.f);
      const float var = rbf(fmaxf(__builtin_fmaf(-mean, mean, sq * (1.f / 640.f)), 0.f));
      const u32 pk = pk2(mean, __builtin_amdgcn_sqrtf(rbf(var + 1e-5f)));
      *lp<u32>(SY_RS + (lane >> 3) * 4) = pk;
    } else if (wave < nch) {
      f32x4 ka[2][2];
      kgen<1>(pw, ldt, wave == 0 ? RB + 16384 : RB + 49152 + (wave - 1) * 12288, lane, ka);
#pragma unroll
      for (int i = 0; i < 4; i++) *lp<f32x4>(L_SSM + wave * SSM_SZ + 512 + i * 1024 + vl) = ka[i >> 1][i & 1];
    }
    lds_set(SY_XF + wave * 4, 1);
    STAMP(1);
    const u32 h = lane >> 5;
    u32 ax[4];
#pragma unroll
    for (int ks = 0; ks < 4; ks++) ax[ks] = opq(ldx + (lane & 31) * 128 + (((2 * ks + h) ^ ((lane >> 1) & 7)) << 4));
    const u32 aw = opq(RB + 49152 + (8 * h + tq) * 192 + (gam & 1) * 32 + tp * 8);
    f32x16 acc[3];
#pragma unroll
    for (int ft = 0; ft < 3; ft++)
#pragma unroll
      for (int i = 0; i < 16; i++) acc[ft][i] = 0.f;
    u32x4 Xf[2][4];
    u32x2 Wf[2][4][3][2];
    auto rd = [&](int s, int i) {
      const int ks = i / 7, r = i % 7;
      if (r == 6) Xf[s & 1][ks] = *lp<u32x4>(ax[ks] + ((s + 2) % 3) * 16384);
      else Wf[s & 1][ks][r >> 1][r & 1] = trd(aw + ((s + 2) % 4) * 12288 + (ks * 16 + (r & 1) * 4) * 192 + (r >> 1) * 64);
    };
    auto mm1 = [&](int s, int i) {
      const int ks = s < 9 ? i / 3 : i % 4, ft = s < 9 ? i % 3 : i / 4;
      acc[ft] = mf32(frag(Wf[s & 1][ks][ft][0], Wf[s & 1][ks][ft][1]), fragv(Xf[s & 1][ks]), acc[ft]);
    };
    chk4(ldflags4(SY_F0), SY_F0, 1);
    chk1(ldflag(SY_XF), SY_XF, 1);
    STAMP(2);
#pragma unroll
    for (int i = 0; i < 28; i++) { rd(0, i); if (i % 7 == 6) pieceX(i / 7, 2); }
    u32 fl = ldflag(SY_F + 4);
    lds_set(sym, 1);
    __builtin_amdgcn_s_setprio(M_PRIO);
    const rsrc_t rw2 = mk(w2, 1536 * 1280);
    const u32 v2 = (lane >> 3) * 1280 + (((lane & 7) ^ (((lane >> 4) & 3) << 1)) << 4);
    const u32 so2b = ((wave >> 1) * 768 + (wave & 1) * 32) * 1280 + qn * 128;
    const u32 so2x = WS_W2X + qn * 49152 + ((wave >> 1) * 768 + (wave & 1) * 32) * 32;
    f32x4 cs[12], bb[12];
    f32x2 st;
    sfor<10>([&](auto S_) {
      constexpr int s = S_;
      if (s == 9) STAMP(4);
      if (s == 4) STAMP(11);
      chk1(fl, s < 9 ? SY_F + (s + 1) * 4 : SY_EPI1, 1);
      if (s > 0 && s < 9) VMCNT(4);
      SB();
#pragma unroll
      for (int gi = 0; gi < 12; gi++) {
        mm1(s, gi);
        if (gi < 8) {
          if (s < 9) {
#pragma unroll
            for (int i = (gi * 7) / 2; i < ((gi + 1) * 7) / 2; i++) rd(s + 1, i);
          } else {
            const u32 cb = opq(L_CS + h * 16);
            if (gi == 0) st = *lp<f32x2>(L_ST + (wave * 32 + (lane & 31)) * 8);
#pragma unroll
            for (int i = gi * 3; i < gi * 3 + 3; i++) { if (i & 1) bb[i >> 1] = *lp<f32x4>(cb + 384 + (i >> 1) * 32); else cs[i >> 1] = *lp<f32x4>(cb + (i >> 1) * 32); }
          }
        }
        if (gi < 4) {
          if (s < 7) pieceX(gi, s + 3);
          else dma(ldx + (s - 7) * 16384 + gi * 1024, v2, rw2, so2b + (ck(s - 7) * 64 + gi * 8) * 1280);
        }
        if (gi == 9 && s == 9) dma_flag(SY_F + 64 + wave * 4, rc);
        if (gi == 7 && s < 9) lds_set(sym, s + 2);
        if (gi == 9 && s < 9) fl = ldflag(s < 8 ? SY_F + (s + 2) * 4 : SY_EPI1);
        SB();
      }
    });
    __builtin_amdgcn_s_setprio(2);
    STAMP(3);
    {
      const u32 vo = WS_ACT + ((pm * 4 + wave) * 48 + pn * 3) * 2048 + lane * 16;
#pragma unroll
      for (int it = 0; it < 6; it++) {
        const int ft = it >> 1, eta = it & 1;
        u32x4 o;
#pragma unroll
        for (int p = 0; p < 4; p++) {
          const int ci = ft * 4 + eta * 2 + (p >> 1), j = (p & 1) * 2;
          const f32x2 two = {2.f, 2.f}, one = {1.f, 1.f};
          f32x2 hh = __builtin_elementwise_fma(st.xx, f32x2{acc[ft][eta * 8 + 2 * p], acc[ft][eta * 8 + 2 * p + 1]},
                                               __builtin_elementwise_fma(st.yy, f32x2{cs[ci][j], cs[ci][j + 1]}, f32x2{bb[ci][j], bb[ci][j + 1]}));
          f32x2 e = {__builtin_amdgcn_exp2f(hh.x), __builtin_amdgcn_exp2f(hh.y)};
          f32x2 dd = __builtin_elementwise_fma(e, e + two, two);
          f32x2 rr = {__builtin_amdgcn_rcpf(dd.x), __builtin_amdgcn_rcpf(dd.y)};
          f32x2 ac = hh * __builtin_elementwise_fma(-two, rr, one);
          o[p] = pk2(ac.x, ac.y);
        }
        st16c(o, rws, vo, it * 1024);
      }
    }
    lds_set(sym, 13);
    const u32x2 tag = *lp<u32x2>(SY_TAG);
    STAMP(5);
    __builtin_amdgcn_s_setprio(3);
    const u32 m2 = wave & 1, kh = wave >> 1;
    const u32 eta2 = gam >> 1, h2 = gam & 1;
    u32 bw[4], bx;
    {
      const u32 row = eta2 * 16 + h2 * 4 + tq, sg = (2 * h2 + (tq >> 1)) & 3;
#pragma unroll
      for (int nt = 0; nt < 4; nt++) bw[nt] = opq(RB + kh * 8192 + row * 128 + ((nt ^ sg) << 5) + tp * 8);
      bx = opq(RB + 81920 + kh * 2048 + row * 32 + tp * 8);
    }
    u32x2 W2[24][5][2];
    u32 bt[5];
    auto rd2 = [&](int k, int i) {
      const int c = k >> 1, sl = c < 5 ? c : c < 8 ? c - 5 : c - 7, kk = k & 1, nt = i >> 1, t = i & 1;
      const u32 in = nt < 4 ? kk * 4096 + t * 1024 : kk * 1024 + t * 256;
      if (c == 8 || c == 9) {
        if (k == 16 && t == 0) bt[nt] = opq(nt < 4 ? bw[nt] - RB : bx - RB - 81920 + 32768);
        W2[k][nt][t] = trd(bt[nt] + (c - 8) * (nt < 4 ? 16384 : 4096) + in);
      } else if (sl == 4 && nt < 4) {
        if (kk == 0 && t == 0) bt[nt] = opq(bw[nt] + 65536);
        W2[k][nt][t] = trd(bt[nt] + in);
      } else W2[k][nt][t] = trd(nt < 4 ? bw[nt] + sl * 16384 + in : bx + sl * 4096 + in);
    };
    chk4(ldflags4(SY_F + 64), SY_F + 64, 1);
    chk1(ldflag(SY_F + 44), SY_F + 44, 1);
#pragma unroll
    for (int i = 0; i < 60; i++) rd2(i / 10, i % 10);
    lds_set(sym, 14);
    publish<1>(rws, WS_FB + ((pm * 4 + wave) * 16 + pn) * 64, tag, lane);
    STAMP(6);
    __builtin_amdgcn_s_setprio(0);
    const u32 rl = m2 * 32 + kh * 16;
    { u32 po[1] = {lane < 8 ? lane * 64 : OOR}; __builtin_amdgcn_s_sleep(B_DELAY); poll_flags<1, B_SLEEP>(rws, po, WS_FB + ((qm * 2 + m2) * 16 + kh * 8) * 64, tag); }
    STAMP(7);
    const u32 va = eta2 * 1024 + (h2 * 32 + c16) * 16 + kh * 256, vb = va ^ 256;
    const u32 soa0 = WS_ACT + ((qm * 2 + m2) * 48 + kh * 24) * 2048;
    const u32 soa = soa0 + rot * 12 * 2048, soa2 = soa0 - rot * 12 * 2048;
    constexpr int WIN = G2_WIN;
    u32x4 af[32][2];
#pragma unroll
    for (int k = 0; k < WIN; k++) { af[k][0] = ld16c(rws, va, soa + k * 2048); af[k][1] = ld16c(rws, vb, soa + k * 2048); }
    f32x4 c2[5][2];
#pragma unroll
    for (int nt = 0; nt < 5; nt++) { c2[nt][0] = f32x4{0, 0, 0, 0}; c2[nt][1] = f32x4{0, 0, 0, 0}; }
    SB();
    u32 ff = 0;
    u32 ea[14], ln = 0;
    auto eaddr = [&](int i) {
      const u32 c16 = ln & 15, gam = ln >> 4, tq = (ln >> 2) & 3;
      if (i == 0) ln = opq(__builtin_amdgcn_mbcnt_hi(~0u, __builtin_amdgcn_mbcnt_lo(~0u, opq(0u))));
      if (i == 1) ea[0] = opq(L_SIMG + (ln >> 2) * 128 + ((((rl >> 4) ^ (tq >> 1) ^ ((gam & 1) << 1)) & 3) << 5) + (ln & 3) * 8);
      if (i == 2) ea[13] = opq((rl + c16) * 128 + (((gam >> 1) ^ (c16 & 7)) << 4) + (gam & 1) * 8);
      if (i >= 3 && i < 7) ea[i - 2] = opq((ea[13] ^ ((i - 3) * 32)) + L_XS);
      if (i == 7) ea[5] = opq(L_XS + 8192 + (rl + c16) * 32 + gam * 8);
      if (i == 8) ea[6] = opq(L_RED + kh * 16384 + m2 * 5120 + ln * 16);
      if (i == 9) ea[7] = opq(L_RED + (kh ^ 1) * 16384 + m2 * 5120 + ln * 16);
      if (i == 10) ea[8] = opq(L_XS + (rl + (ln >> 3)) * 128 + (((ln & 7) ^ (ln >> 3)) << 4));
      if (i == 11) ea[9] = opq(L_XS + 8192 + rl * 32 + (ln & 31) * 16);
      if (i == 12) ea[10] = opq((qm * 64 + rl + (ln >> 3)) * 1280 + (ln & 7) * 16 + qn * 128);
      if (i == 13) ea[11] = opq(ea[10] + 8 * 1280);
      if (i == 14) ea[12] = opq(ln < 32 ? (qm * 64 + rl + (ln >> 1)) * 1280 + (ln & 1) * 16 + 1024 + qn * 32 : OOR);
    };
    u32x2 sv[5], xv[5];
    float sf[20], xf[20];
    sfor<24>([&](auto K_) {
      constexpr int k = K_;
      constexpr int kr = k < 4 ? k + 6 : k >= 8 && k < 22 ? k + 2 : -1;
      if (k == 7 || k == 13 || k == 17) ff = ldflag(k == 7 ? SY_F + 48 : k == 13 ? SY_FULL : SY_F + 52);
      if (k == 8 || k == 14 || k == 18) chk1(ff, k == 8 ? SY_F + 48 : k == 14 ? SY_FULL : SY_F + 52, 1);
      if (k == 20) ff = ldflag(SY_EPI2);
      if (k == 21) { STAMP(8); chk1(ff, SY_EPI2, 1); STAMP(9); }
      SB();
#pragma unroll
      for (int j = 0; j < 10; j++) {
        const int i = k < 23 ? j : j < 5 ? 2 * j + 1 : 2 * (j - 5);
        c2[i >> 1][i & 1] = mf16(frag(W2[k][i >> 1][0], W2[k][i >> 1][1]), fragv(af[k][i & 1]), c2[i >> 1][i & 1]);
        if (kr >= 0) { if (k >= 8) rd2(kr, i); else if (i >= 2) { rd2(kr, i - 2); if (i >= 8) rd2(kr, i); } }
        if (k >= 1 && k - 1 + WIN < 24 && (i == 0 || i == 5)) af[k - 1 + WIN][i / 5] = ld16c(rws, i ? vb : va, (k - 1 + WIN < 12 ? soa : soa2) + (k - 1 + WIN) * 2048);
        if (k >= 5 && k < 8 && (j & 1) == 0) eaddr((k - 5) * 5 + (j >> 1));
        if (k == 21) { if (j & 1) xv[j >> 1] = *lp<u32x2>(ea[1 + (j >> 1)]); else sv[j >> 1] = trd(ea[0] + (j >> 1) * 2048); }
        if (k >= 22) {
          const u32 v = k == 22 ? sv[j >> 1][j & 1] : xv[j >> 1][j & 1];
          float lo = bflo(v), hi = bfhi(v);
          asm volatile("" : "+v"(lo), "+v"(hi));
          if (k == 22) { sf[2 * j] = lo; sf[2 * j + 1] = hi; } else { xf[2 * j] = lo; xf[2 * j + 1] = hi; }
        }
        if (k == 23 && j >= 5) *lp<f32x4>(ea[6] + (j - 5) * 1024) = c2[j - 5][1];
        SB();
      }
      if (k == 3) lds_set(sym, 16);
      if (k == 9) lds_set(sym, 17);
      if (k == 13) lds_set(sym, 18);
    });
    lds_set(SY_RED + wave * 4, 1);
    chk1(ldflag(SY_RED + (wave ^ 2) * 4), SY_RED + (wave ^ 2) * 4, 1);
    {
      f32x4 pv[5];
#pragma unroll
      for (int nt = 0; nt < 5; nt++) pv[nt] = *lp<f32x4>(ea[7] + nt * 1024);
      SB();
#pragma unroll
      for (int nt = 0; nt < 5; nt++) {
        u32x2 r;
#pragma unroll
        for (int j = 0; j < 2; j++) {
          const int e = nt * 4 + j * 2;
          const f32x2 o = f32x2{pv[nt][2 * j], pv[nt][2 * j + 1]} + f32x2{c2[nt][0][2 * j], c2[nt][0][2 * j + 1]};
          const f32x2 dn = f32x2{__builtin_amdgcn_exp2f(-o.x), __builtin_amdgcn_exp2f(-o.y)} + f32x2{1.f, 1.f};
          const f32x2 rc_ = {__builtin_amdgcn_rcpf(dn.x), __builtin_amdgcn_rcpf(dn.y)};
          const f32x2 y = __builtin_elementwise_fma(o * f32x2{sf[e], sf[e + 1]}, rc_, f32x2{xf[e], xf[e + 1]});
          r[j] = pk2(y.x, y.y);
        }
        *lp<u32x2>(ea[1 + nt]) = r;
      }
      CFENCE();
      const rsrc_t ro = mk(out, 2048 * 1280);
      const u32x4 v0 = *lp<u32x4>(ea[8]), v1 = *lp<u32x4>(ea[8] + 1024), v2_ = *lp<u32x4>(ea[9]);
      st16(v0, ro, ea[10], 0);
      st16(v1, ro, ea[11], 0);
      st16(v2_, ro, ea[12], 0);
    }
    STAMP(10);
  } else if (wave2 == 7) {
    __builtin_amdgcn_s_setprio(3);
    const rsrc_t rw2 = mk(w2, 1536 * 1280);
    u32 v2[4];
    auto ldW = [&](u32 s) {
      const u32 so = pn * 192 + (s < 5 ? s + 5 * rot1 : s - 5 * rot1) * (64 * 3072), ld = RB + 49152 + ((s + 2) & 3) * 12288;
#pragma unroll
      for (int i = 0; i < 4; i++) dma3(ld + i * 3072, vw[0], vw[1], vw[2], rw1, so + i * 16 * 3072);
      dma_flag(SY_F + s * 4, rc);
    };
    auto ld2q = [&](u32 j, u32 slot, u32 q) {
      dma4(RB + slot * 16384 + q * 4096, v2[0], v2[1], v2[2], v2[3], rw2, ((q >> 1) * 768 + ck(j) * 64 + (q & 1) * 32) * 1280 + qn * 128);
    };
    auto ld2x = [&](u32 j, u32 slot) {
      dma2c(RB + 81920 + slot * 4096, vl, rws, WS_W2X + qn * 49152 + ck(j) * 2048);
      dma2c(RB + 81920 + slot * 4096 + 2048, vl, rws, WS_W2X + qn * 49152 + 768 * 32 + ck(j) * 2048);
    };
    { const u32 vt = vr + (((lane & 7) ^ (((lane >> 4) & 1) << 1)) << 4); tile(vt, vt ^ 64, tm, tn); }
    const u32x2 ep = ld8c(rws, 0, WS_EPOCH);
    SB();
    VMCNT(1);
    STAMP(9);
    {
      u32x4 t[10];
      const u32 sg = (tq >> 1) | ((gam & 1) << 1);
      const u32 ra = L_XS + (gam * 8 + tq) * 128 + tp * 8, re = opq(L_XS + 8192 + (gam * 8 + tq) * 32 + tp * 8);
#pragma unroll
      for (int cb = 0; cb < 5; cb++) {
        const u32 a = cb < 4 ? opq(ra + ((cb ^ sg) << 5)) : re;
#pragma unroll
        for (int hf = 0; hf < 2; hf++) {
          u32x2 a0 = trd(a + hf * (cb < 4 ? 4096 : 1024)), a1 = trd(a + hf * (cb < 4 ? 4096 : 1024) + (cb < 4 ? 512 : 128));
          t[cb * 2 + hf] = u32x4{a0.x, a0.y, a1.x, a1.y};
        }
      }
      CFENCE(); SB();
      const u32 wa0 = opq(L_XS + c16 * 128 + ((gam ^ (c16 & 7)) << 4)), wa1 = opq(wa0 ^ 64);
#pragma unroll
      for (int cb = 0; cb < 5; cb++) { *lp<u32x4>(wa0 + cb * 2048) = t[cb * 2]; *lp<u32x4>(wa1 + cb * 2048) = t[cb * 2 + 1]; }
      *lp<u32x4>(SY_TAG) = u32x4{ep.x + 1, ep.y + 0x9e3779b9u, 1u, 0u};
      CFENCE(); SB();
      const u32 rb = opq(L_XS + (lane >> 3) * 128 + (((lane & 7) ^ (lane >> 3)) << 4));
#pragma unroll
      for (int i = 0; i < 10; i++) t[i] = *lp<u32x4>(rb + i * 1024);
      SB();
      const u32 vo = (lane >> 3) * 4096 + (lane & 7) * 16 + WS_XT + tm * 128;
#pragma unroll
      for (int i = 0; i < 10; i++) st16c(t[i], rws, vo, (i < 8 ? tn * 64 + i * 8 : 512 + tn * 16 + (i - 8) * 8) * 4096);
      SB();
    }
    STAMP(10);
    lds_spin4(SY_XF, 1);
    st16c(*lp<u32x4>(SY_RS + (lane & 1) * 16), rws, lane < 2 ? WS_STG + lane * 4096 + (pm * 16 + (pn ^ 8)) * 16 : OOR, 0);
    {
      const u32x2 rs = *lp<u32x2>(SY_RS + (lane & 3) * 8);
      st16c(u32x4{rs.x, ep.x + 1, rs.y, ep.y + 0x9e3779b9u}, rws, lane < 4 ? WS_REC + (pm * 16 + (pn ^ 8)) * 128 + lane * 16 : OOR, 0);
    }
    SB();
    ldW(2); ldW(3);
    VMCNT(26);
    st8c(u32x2{ep.x + 1, ep.y + 0x9e3779b9u}, rws, lane == 0 ? 0 : OOR, WS_F1 + g8 * REP + r5 * 64);
    STAMP(1);
#pragma unroll
    for (int j = 0; j < 4; j++) v2[j] = vr + (((lane & 7) ^ (((lane >> 4) & 3) << 1)) << 4) + j * 9216;
#pragma unroll 1
    for (u32 s = 4; s < 10; s++) { lds_spin4(SY_M, s - 3); ldW(s); }
    STAMP(5);
    {
      const u32x2 tag = {ep.x + 1, ep.y + 0x9e3779b9u};
      const u32x2 bv = *(const u32x2*)(b1 + pn * 96 + min(lane, 23) * 4);
      u32x4 g[6];
#pragma nounroll
      for (u32 spin = 0; spin < SPIN_CAP; spin++) {
        CFENCE();
        g[0] = ld16c(rws, (lane >> 2) * 128 + (lane & 3) * 16, WS_REC + pm * 2048);
#pragma unroll
        for (int k = 0; k < 5; k++) g[1 + k] = ld16c(rws, min(lane, 47) * 16, WS_CSP + k * 12288 + pn * 768);
        bool ok = true;
#pragma unroll
        for (int k = 0; k < 6; k++) ok = ok && g[k].y == tag.x && g[k].w == tag.y;
        if (__builtin_amdgcn_ballot_w64(!ok) == 0) break;
        __builtin_amdgcn_s_sleep(8);
      }
      CFENCE();
      STAMP(8);
      const float r0 = LOG2E * __builtin_amdgcn_rcpf(bfhi(g[0].x)), r1 = LOG2E * __builtin_amdgcn_rcpf(bfhi(g[0].z));
      *lp<f32x4>(L_ST + lane * 16) = f32x4{r0, -bflo(g[0].x) * r0, r1, -bflo(g[0].z) * r1};
      f32x2 cs = {0.f, 0.f};
#pragma unroll
      for (int k = 0; k < 5; k++) cs += f32x2{__uint_as_float(g[1 + k].x), __uint_as_float(g[1 + k].z)};
      if (lane < 48) *lp<f32x2>(L_CS + lane * 8) = cs;
      if (lane < 24) *lp<f32x4>(L_B1 + lane * 16) = f32x4{bflo(bv.x) * LOG2E, bfhi(bv.x) * LOG2E, bflo(bv.y) * LOG2E, bfhi(bv.y) * LOG2E};
      lds_set(SY_EPI1, 1);
    }
    { const u32 vs = vr + (((lane & 7) ^ (lane >> 3)) << 4); tile(vs, vs, qm, qn); }
    STAMP(2);
    lds_spin4(SY_M, 7);
    ld2q(3, 3, 0); ld2q(3, 3, 1); ld2q(3, 3, 2);
    lds_spin4(SY_M, 8);
    ld2q(3, 3, 3); ld2q(4, 4, 0); ld2q(4, 4, 1);
    lds_spin4(SY_M, 9);
    ld2q(4, 4, 2); ld2q(4, 4, 3);
    lds_spin4(SY_M, 10);
    lds_wait(SY_W2X, 1);
#pragma unroll 1
    for (u32 j = 0; j < 5; j++) ld2x(j, j);
    dma_flag(SY_F + 44, rc);
    STAMP(3);
    u32x4 pk_[2][20];
#pragma unroll
    for (int j = 0; j < 2; j++)
#pragma unroll
      for (int i = 0; i < 20; i++) {
        if (i < 16) pk_[j][i] = ld16(rw2, v2[0], ((i >> 3) * 768 + ck(8 + j) * 64 + (i & 7) * 8) * 1280 + qn * 128);
        else pk_[j][i] = ld16c(rws, vl, WS_W2X + qn * 49152 + (((i - 16) >> 1) * 768 + ck(8 + j) * 64) * 32 + (i & 1) * 1024);
      }
    SB();
    STAMP(4);
    lds_spin4(SY_M, 14);
#pragma unroll 1
    for (u32 j = 0; j < 3; j++) {
#pragma unroll
      for (int q = 0; q < 4; q++) ld2q(5 + j, j, q);
      ld2x(5 + j, j);
    }
    dma_flag(SY_F + 48, rc);
    lds_wait4(SY_SDONE, 1);
    const u32 wb = opq(L_SSM + vl);
#pragma unroll
    for (int j = 0; j < 2; j++)
#pragma unroll
      for (int i = 0; i < 20; i++) *lp<u32x4>(wb + (i < 16 ? j * 16384 + i * 1024 : 32768 + j * 4096 + (i - 16) * 1024)) = pk_[j][i];
    lds_set(SY_FULL, 1);
    STAMP(6);
    lds_wait4(SY_M, 16);
#pragma unroll 1
    for (u32 j = 0; j < 2; j++) {
#pragma unroll
      for (int q = 0; q < 4; q++) ld2q(10 + j, 3 + j, q);
      ld2x(10 + j, 3 + j);
    }
    dma_flag(SY_F + 52, rc);
    VMCNT(0);
    STAMP(7);
  } else if (wave2 - 4 < nch) {
    const u32 LU = L_SSM + (wave - 4) * SSM_SZ, LB = LU + 4608;
    __builtin_amdgcn_s_setprio(S_PRIO);
    STAMP(1);
    f32x4 ka[2][2];
    kgen<2>(pw, ldt, LB, lane, ka);
    STAMP(2);
    const u32 fk = ldflag(SY_XF + (wave - 4) * 4);
    u32x4 tb[8];
    {
      const u32 dum = LB + LB_DUM + lane * 8;
      const bool g2 = gam == 2, g3 = gam == 3, lo3 = g3 && c16 < 2;
      const u32 kb = LB + 32 + 16 * c16 + (gam < 2 ? 8 * gam : g2 ? 512 : 1024), t4 = LB + T4 + 116 + 4 * c16;
      tb[4] = u32x4{(!g3 || lo3) ? kb : dum, (g2 || lo3) ? kb + 8 : g3 ? t4 : dum, g2 ? kb + 12 : g3 ? t4 + 128 : dum, ((g2 && c16 > 0) || lo3) ? kb - 2 : dum};
      tb[5] = u32x4{!g3 ? kb + 256 : dum, g2 ? kb + 264 : g3 ? t4 + 64 : dum, g2 ? kb + 268 : g3 ? t4 + 192 : dum, g2 ? kb + 254 : dum};
      tb[6] = u32x4{gam < 2 ? 0x03020100u : 0x01000100u, gam < 2 ? 0x07060504u : g2 ? 0x05040302u : 0x03020100u,
                    g2 ? 0x05040504u : c16 < 2 ? 0x03020302u : 0x03020100u, g2 ? 0x07060706u : 0x07060504u};
      tb[7] = u32x4{g2 ? 0x05040504u : 0x03020100u,
                    lane == 0 ? LB + (16 + 518) * 2 : lane == 1 ? LB + T4 + 186 * 2 : lane < 11 ? LB + (16 + 577 + lane) * 2 : lane < 13 ? LB + T8 + (18 + lane) * 2 : dum,
                    0u, LB + lane * 16 + (lane < 2 ? 0 : 1024)};
      asm volatile("" : "+v"(tb[4]), "+v"(tb[5]), "+v"(tb[6]), "+v"(tb[7]));
      SB();
    }
    if (lane < 32) *lp<u32x4>(LU + lane * 16) = u32x4{0, 0, 0, 0};
    const u32 lb16 = opq(LB + lane * 16);
    *lp<u32x4>(tb[7].w) = u32x4{0, 0, 0, 0};
    *lp<u32x4>(lb16 + T4) = u32x4{0, 0, 0, 0};
    CFENCE();
    chk1(fk, SY_XF + (wave - 4) * 4, 1);
#pragma unroll
    for (int i = 0; i < 4; i++) ka[i >> 1][i & 1] += *lp<f32x4>(LU + 512 + i * 1024 + vl);
    lds_set(SY_KD + (wave - 4) * 4, 1);
    if (wave == 4) lds_set(SY_KD + 12, 1);
    if (lane == 0) ka[0][0][0] += bflo(dsk) * LN2;
    {
      u32 a01[2], a23[2], b01[2], b23[2];
#pragma unroll
      for (int qb = 0; qb < 2; qb++) {
        a01[qb] = pk2(ka[0][qb][0], ka[0][qb][1]); a23[qb] = pk2(ka[0][qb][2], ka[0][qb][3]);
        b01[qb] = pk2(ka[1][qb][0], ka[1][qb][1]); b23[qb] = pk2(ka[1][qb][2], ka[1][qb][3]);
      }
      const u32 t8a = opq(LB + T8 + (29 + c16 + 128 * gam) * 2);
#pragma unroll
      for (int qb = 0; qb < 2; qb++) {
        *lp<u32x2>(tb[4 + qb].x) = u32x2{__builtin_amdgcn_perm(a23[qb], a01[qb], tb[6].x), __builtin_amdgcn_perm(a23[qb], a01[qb], tb[6].y)};
        *lp<u32>(tb[4 + qb].y) = __builtin_amdgcn_perm(a23[qb], a01[qb], qb ? tb[7].x : tb[6].z);
        *lp<u32>(tb[4 + qb].z) = __builtin_amdgcn_perm(a23[qb], a01[qb], tb[6].w);
        *lp<u16>(t8a + qb * 32) = (u16)b01[qb];
        *lp<u16>(t8a + qb * 32 + 64) = (u16)(b01[qb] >> 16);
        *lp<u16>(t8a + qb * 32 + 128) = (u16)b23[qb];
        *lp<u16>(t8a + qb * 32 + 192) = (u16)(b23[qb] >> 16);
      }
      CFENCE();
      *lp<u16>(tb[4].w) = (u16)a01[0];
      *lp<u16>(tb[5].w) = (u16)a01[1];
      const u32 k520 = __builtin_amdgcn_readlane(a01[0], 49), k1024 = __builtin_amdgcn_readlane(b01[0], 0), k1032 = __builtin_amdgcn_readlane(b01[0], 1);
      *lp<u16>(tb[7].y) = (u16)(lane == 0 ? k520 : lane < 4 ? k1024 : lane < 11 ? k1032 : 0u);
    }
    CFENCE();
    {
      const u32 lb2 = opq(LB + (64 + min((u32)lane, 13u)) * 16);
      u32x4 wv[2]; u32x2 yv[2];
      wv[0] = *lp<u32x4>(lb16); yv[0] = *lp<u32x2>(lb16 - 8); wv[1] = *lp<u32x4>(lb2); yv[1] = *lp<u32x2>(lb2 - 8);
      SB();
#pragma unroll
      for (int ps = 0; ps < 2; ps++) {
        const u32 b = ps ? lb2 : lb16;
        const u32x4 a = wv[ps]; const u32x2 y = yv[ps];
        const u32 s0 = __builtin_amdgcn_alignbit(y.y, y.x, 16), s1 = __builtin_amdgcn_alignbit(a.x, y.y, 16), s2 = __builtin_amdgcn_alignbit(a.y, a.x, 16),
                  s3 = __builtin_amdgcn_alignbit(a.z, a.y, 16), s4 = __builtin_amdgcn_alignbit(a.w, a.z, 16);
        *lp<u32x4>(b + 1344) = u32x4{s1, s2, s3, s4};
        *lp<u32x4>(b + 2688) = u32x4{y.y, a.x, a.y, a.z};
        *lp<u32x4>(b + 4032) = u32x4{s0, s1, s2, s3};
      }
      CFENCE();
    }
    bf16x8 Ff[18], H4[6], H8[4];
    {
      const u32 ha = opq(LB + T4 + (4 * c16 + 8 * gam) * 2), ha2 = opq(ha + 8);
#pragma unroll
      for (int e = 0; e < 6; e++) H4[e] = frag(*(volatile LAS u32x2*)lp<u32x2>(ha + e * 64), *(volatile LAS u32x2*)lp<u32x2>(ha2 + e * 64));
      const u32 hb = opq(LB + T8 + (2 * c16 + 8 * gam) * 2);
#pragma unroll
      for (int e = 0; e < 4; e++) {
        u32x4 v;
#pragma unroll
        for (int j = 0; j < 4; j++) v[j] = *(volatile LAS u32*)lp<u32>(hb + e * 64 + j * 4);
        H8[e] = fragv(v);
      }
      const u32 fa = opq(LB + tq * 1344 + (28 - 8 * gam + 4 * tp) * 2);
#pragma unroll
      for (int m = 0; m < 18; m++) { const int mm = m < 17 ? m : 18; Ff[m] = frag(trd(fa + mm * 64 + 8), trd(fa + mm * 64)); }
    }
    CFENCE();
    *lp<u32x4>(LB + (lane == 0 ? 0 : lane < 9 ? S5 + 3072 - 16 : lane < 16 ? S3 + 3072 - 144 : lane < 18 ? S9 + 2048 - 256 : S7 + 2048 - 288) + min(lane, 19) * 16) = u32x4{0, 0, 0, 0};
#pragma unroll
    for (int wd = 0; wd < 4; wd++) {
      const u32 msk = ((((1u << (2 * wd + 3)) - 1u) << (c16 + 7 - wd)) >> (8 * gam)) & 255u, m2 = msk | (msk << 15);
#pragma unroll
      for (int j = 0; j < 4; j++) tb[wd][j] = __umul24((m2 >> (2 * j)) & 0x10001u, 0x3f80u);
      asm volatile("" : "+v"(tb[wd]));
    }
    STAMP(3);
    if (wave < 6) {
      u32x4 sa[2][2], xa[3][2];
      if (wave == 4) {
        u32 po[1] = {lane < 8 ? lane * 128 : OOR};
        const u32x2 tg = get_tag();
        poll_flags<1, A_SLEEP>(rws, po, WS_FA + g8 * REP, tg);
        lds_set(SY_W2X, 1);
        if (w == 0) st8c(tg, rws, lane == 0 ? 0 : OOR, WS_EPOCH);
      } else lds_wait(SY_W2X, 1);
      lds_wait4(SY_KD, 1);
      STAMP(10);
      __builtin_amdgcn_s_setprio(3);
#pragma unroll
      for (int i = 0; i < 2; i++) {
#pragma unroll
        for (int j = 0; j < 2; j++) sa[i][j] = ld16c(rws, lane * 16, WS_STG + j * 4096 + ((wave - 4) * 2 + i) * 1024);
#pragma unroll
        for (int c = 0; c < 3; c++) xa[c][i] = ld16c(rws, lane * 16, WS_XT + min(ch0 + c, 639u) * 4096 + ((wave - 4) * 2 + i) * 1024);
      }
      SB();
      __builtin_amdgcn_s_setprio(S_PRIO);
      const u32 ub = opq(L_SSM + 512 + (wave - 4) * 2048 + lane * 16);
#pragma unroll
      for (int i = 0; i < 2; i++) {
        u32x4 o[3];
#pragma unroll
        for (int j = 0; j < 4; j++) {
          const u32 p0 = sa[i][j >> 1][(j & 1) * 2], p1 = sa[i][j >> 1][(j & 1) * 2 + 1];
          const float r0 = __builtin_amdgcn_rcpf(bfhi(p0)), r1 = __builtin_amdgcn_rcpf(bfhi(p1));
          const float m0 = -bflo(p0) * r0, m1 = __uint_as_float(opq(__float_as_uint(-bflo(p1) * r1)));
#pragma unroll
          for (int c = 0; c < 3; c++) {
            float u0;
            asm("v_fma_f32 %0, %1, %2, %3" : "=v"(u0) : "v"(bflo(xa[c][i][j])), "v"(r0), "v"(m0));
            o[c][j] = pk2(u0, __builtin_fmaf(bfhi(xa[c][i][j]), r1, m1));
          }
        }
#pragma unroll
        for (int c = 0; c < 3; c++) *lp<u32x4>(ub + c * SSM_SZ + i * 1024) = o[c];
      }
      *lp<u32x2>(SY_U + (wave - 4) * 8) = u32x2{1u, 1u};
      CFENCE();
      STAMP(11);
    }
    lds_wait4(SY_U, 1);
    STAMP(7);
    const u32 ub = opq(LU + 32 + 32 * c16 + 16 * gam);
    const u32 g4 = opq(LB + ((tq & 1) ? S3 : S5) - 8 + (108 - 32 * gam - 4 * tq + 4 * tp) * 2);
    const u32 g8a = opq(LB + ((tq & 1) ? S9 : S7) + (216 - 64 * gam - 8 * tq + 4 * tp) * 2);
    const u32 sb = opq(LB + 32 * c16 + 8 * gam);
    constexpr int RING = 8, AHEAD = 6;
    u32x4 uf[RING];
    auto ldf = [&](int n) {
      if (n < 12) { u32x2 a = trd(g4 + n * 256 + 32), b = trd(g4 + n * 256); uf[n % RING] = u32x4{a.x, a.y, b.x, b.y}; }
      else if (n < 16) { u32x2 a = trd(g8a + (n - 12) * 512 + 64), b = trd(g8a + (n - 12) * 512); uf[n % RING] = u32x4{a.x, a.y, b.x, b.y}; }
      else if (n < 80) uf[n % RING] = *lp<u32x4>(ub + (n - 16) * 64);
    };
    {
      u32x4 sf[6];
#pragma unroll
      for (int t = 0; t < 6; t++) sf[t] = *lp<u32x4>(ub + 432 + t * 512);
      SB();
      f32x4 sr_[2][4];
#pragma unroll
      for (int t = 0; t <= 6; t++) {
#pragma unroll
        for (int wd = 0; wd < 4; wd++) {
          if (t < 6 && wd < (t < 4 ? 4 : 2)) sr_[t & 1][wd] = mf16(fragv(tb[wd]), fragv(sf[t]), f32x4{0, 0, 0, 0});
          if (t > 0 && wd < (t - 1 < 4 ? 4 : 2)) {
            const f32x4 s = sr_[(t - 1) & 1][wd];
            *lp<u32x2>(sb + (wd == 0 ? S3 : wd == 1 ? S5 : wd == 2 ? S7 : S9) + (t - 1) * 512) = u32x2{pk2(s[0], s[1]), pk2(s[2], s[3])};
          }
          SB();
        }
      }
    }
    CFENCE();
#pragma unroll
    for (int n = 0; n < AHEAD; n++) ldf(n);
    f32x4 y[8];
#pragma unroll
    for (int c = 0; c < 8; c++) y[c] = f32x4{0, 0, 0, 0};
    const u32 yr = opq(LB + vl), yo = opq(vl + WS_ST + d * 4096);
    const u32x2 tag = *lp<u32x2>(SY_TAG);
    u32x4 ov;
    sfor<88>([&](auto N) {
      constexpr int n = N;
      if (n == YIELD_AT) {
        lds_wait(SY_M + (wave - 4) * 4, 13);
        __builtin_amdgcn_s_setprio(2);
        STAMP(4);
      }
      if constexpr (n < 80) {
        const bf16x8 f = fragv(uf[n % RING]);
        ldf(n + AHEAD);
        sfor<8>([&](auto C) {
          constexpr int c = C;
          if constexpr (n < 12) {
            constexpr int e = 2 * c - n;
            if constexpr (e >= 3 && e <= 8) y[c] = mf16(f, H4[e - 3], y[c]);
          } else if constexpr (n < 16) {
            constexpr int e = c - (n - 12);
            if constexpr (e >= 4) y[c] = mf16(f, H8[e - 4], y[c]);
          } else {
            constexpr int m = 8 * c - (n - 23);
            if constexpr (m >= 0 && m <= 16) y[c] = mf16(Ff[m], f, y[c]);
            if constexpr (m == 32) y[c] = mf16(Ff[17], f, y[c]);
          }
        });
      }
      if constexpr (n >= 25 && (n - 25) % 8 == 0) {
        constexpr int c = (n - 25) / 8;
        *lp<u32x2>(sb + c * 512) = u32x2{pk2(y[c][0], y[c][1]), pk2(y[c][2], y[c][3])};
        CFENCE();
        if (c & 1) ov = *lp<u32x4>(yr + (c >> 1) * 1024);
      }
      if constexpr (n >= 39 && (n - 39) % 16 == 0) st16c(ov, rws, yo, ((n - 39) / 16) * 1024);
      SB();
    });
    STAMP(5);
    lds_set(SY_SDONE + (wave - 4) * 4, 1);
    if (wave == 4) lds_set(SY_SDONE + 12, 1);
    publish<8>(rws, WS_FC + d * 64, tag, lane);
    STAMP(6);
    if (wave == 4) {
      u32 pc_[2] = {(qn * 64 + lane) * 64, lane < 16 ? (512 + qn * 16 + lane) * 64 : OOR};
      lds_wait4(SY_M, C_AT);
      __builtin_amdgcn_s_sleep(C_DELAY);
      poll_flags<2, C_SLEEP>(rws, pc_, WS_FC + g8 * REP, tag);
      STAMP(8);
      u32x4 sv[10];
#pragma unroll
      for (int i = 0; i < 10; i++) {
        const u32 d0 = i < 8 ? qn * 64 + i * 8 : 512 + qn * 16 + (i - 8) * 8;
        sv[i] = ld16c(rws, (lane >> 3) * 4096 + (lane & 7) * 16, WS_ST + d0 * 4096 + qm * 128);
      }
      lds_wait4(SY_M, 17);
      const u32 sb2 = opq(L_SIMG + (lane >> 3) * 128 + (((lane & 7) ^ (((lane >> 4) & 3) << 1)) << 4));
#pragma unroll
      for (int i = 0; i < 10; i++) *lp<u32x4>(sb2 + i * 1024) = sv[i];
      lds_wait4(SY_M, 18);
      lds_set(SY_EPI2, 1);
      STAMP(9);
    }
  } else {
    const rsrc_t rw2 = mk(w2, 1536 * 1280);
    const u32 io = (w >> 4) * 4 + (g8 >> 1);
    const u32 kr = min(io / 12, 4u), lg = (g8 & 1) * 12 + io % 12;
    __builtin_amdgcn_s_setprio(2);
    u32x4 e[3], v[16];
    lds_set(SY_SDONE + 8, 1);
    lds_set(SY_KD + 8, 1);
    __builtin_amdgcn_s_sleep(P_DELAY);
#pragma unroll
    for (int i = 0; i < 3; i++) {
      const u32 c = i * 64 + lane, q = c / 24, rr = c - q * 24;
      e[i] = ld16(rw2, (rr >> 1) * 1280 + q * 32 + (rr & 1) * 16, pair * 12 * 1280 + 1024);
    }
#pragma unroll
    for (int i = 0; i < 16; i++) v[i] = ld16(rw1, io < 60 ? (lane >> 3) * 3072 + (lane & 7) * 16 : OOR, (kr * 128 + i * 8) * 3072 + lg * 128);
    SB();
#define W2_TOUCH() asm volatile("s_mov_b32 m0, %0\n\ts_nop 0\n\tbuffer_load_dword %1, %3, %4 offen lds\n\tbuffer_load_dword %2, %3, %4 offen lds" \
                 ::"s"(L_DUMMY), "v"(vr + (lane & 7) * 128), "v"(lane < 32 ? vr + (lane & 7) * 128 + 8 * 1280 : OOR), "s"(rw2), "s"(pair * 12 * 1280))
#pragma unroll
    for (int i = 0; i < 3; i++) {
      const u32 c = i * 64 + lane, q = c / 24, rr = c - q * 24;
      st16c(e[i], rws, q * 49152 + rr * 16, WS_W2X + pair * 384);
    }
    SB();
    const u32x2 tag = get_tag();
    publish<1>(rws, WS_F1 + g8 * REP + (32 + (w >> 4)) * 64, tag, lane);
    STAMP(4);
    float s[8];
#pragma unroll
    for (int j = 0; j < 8; j++) s[j] = 0.f;
#pragma unroll
    for (int i = 0; i < 16; i++)
#pragma unroll
      for (int j = 0; j < 4; j++) { s[2 * j] += bflo(v[i][j]); s[2 * j + 1] += bfhi(v[i][j]); }
#pragma unroll
    for (int j = 0; j < 8; j++) s[j] = swap32_add(swap16_add(dpp_add<0x128>(s[j])));
    {
      const u32 pt = (lane >> 3) & 3;
      const float a = pt == 0 ? s[0] : pt == 1 ? s[2] : pt == 2 ? s[4] : s[6], b = pt == 0 ? s[1] : pt == 1 ? s[3] : pt == 2 ? s[5] : s[7];
      st16c(u32x4{__float_as_uint(a), tag.x, __float_as_uint(b), tag.y}, rws, (lane < 32 && io < 60) ? (lane & 7) * 64 + pt * 16 + WS_CSP + kr * 12288 + lg * 512 : OOR, 0);
    }
    STAMP(1);
    if (r5 == 31) {
      u32 po[1] = {lane < 48 ? lane * 64 : OOR};
      __builtin_amdgcn_s_sleep(G_DELAY);
      poll_flags<1, G_SLEEP>(rws, po, WS_F1 + g8 * REP, tag);
      publish<8>(rws, WS_FA + g8 * 128, tag, lane);
      STAMP(3);
    }
    VMCNT(0);
    STAMP(2);
  }
  STAMPS_OUT();
}
extern "C" __device__ __attribute__((used, noinline)) void s4mlp_end() {}

extern "C" void kernel_launch(void* const* d_in, const int* in_sizes, int n_in, void* d_out, int out_size, void* d_ws, size_t ws_size,
                              hipStream_t stream) {
  s4mlp<<<256, 512, 0, stream>>>((const u16*)d_in[0], (const u16*)d_in[1], (const u16*)d_in[2], (const u16*)d_in[3], (const u16*)d_in[4],
                                 (const u16*)d_in[5], (const u16*)d_in[6], (const u16*)d_in[7], (const u16*)d_in[8], (const u16*)d_in[10],
                                 (const u16*)d_in[11], (const u16*)d_in[12], (u16*)d_out, (char*)d_ws);
}
```
